```python
import torch

def _softmax(x, axis=-1):
    return torch.softmax(x, dim=axis)
def _gelu(x):
    return torch.nn.functional.gelu(x, approximate="tanh")
def _silu(x):
    return torch.nn.functional.silu(x)
def _sigmoid(x):
    return torch.sigmoid(x)
def _layernorm(x, axis=-1, eps=1e-5):
    m = x.mean(dim=axis, keepdim=True)
    v = x.var(dim=axis, keepdim=True, unbiased=False)
    return (x - m) / torch.sqrt(v + eps)
def _scatter_add(tgt, idx, src, axis=0):
    return tgt.clone().index_add_(axis, idx, src)
def _segment_sum(src, seg, n):
    out = torch.zeros((n,) + src.shape[1:], dtype=src.dtype)
    return out.index_add_(0, seg, src)
def _pad(x, pw):
    flat = tuple(v for pair in reversed(pw) for v in pair)
    return torch.nn.functional.pad(x, flat)

kernel_name = 'concat_heads__mha_block_cached__mha_block_cached__near_ridge'

def setup_inputs():

    _w = (lambda a: (lambda t: t.to(torch.bfloat16) if t.is_floating_point() else t)(torch.as_tensor(a)))
    def _A_setup():
        B, Sq, D = 8, 128, 576
        H, Sk, Dh = 32, 1024, 128
        return {
            "x": _w((lambda *s: torch.randn(*s, dtype=torch.bfloat16) * 0.8)(B, Sq, D)),
            "k_cache": _w((lambda *s: torch.randn(*s, dtype=torch.bfloat16) * 0.8)(B, H, Sk, Dh)),
            "v_cache": _w((lambda *s: torch.randn(*s, dtype=torch.bfloat16) * 0.05)(B, H, Sk, Dh)),
            "wq": _w((lambda *s: torch.randn(*s, dtype=torch.bfloat16) * 0.8)(D, H * Dh) * (D ** -0.5)),
            "wo": _w((lambda *s: torch.randn(*s, dtype=torch.bfloat16) * 0.05)(H * Dh, D) * ((H * Dh) ** -0.5)),
        }
    _A_in = _A_setup()

    def _B_setup():
        B, Sq, D = 8, 128, 576
        H, Sk, Dh = 32, 1024, 128
        return {
            "x": _w((lambda *s: torch.randn(*s, dtype=torch.bfloat16) * 0.8)(B, Sq, D)),
            "k_cache": _w((lambda *s: torch.randn(*s, dtype=torch.bfloat16) * 0.8)(B, H, Sk, Dh)),
            "v_cache": _w((lambda *s: torch.randn(*s, dtype=torch.bfloat16) * 0.05)(B, H, Sk, Dh)),
            "wq": _w((lambda *s: torch.randn(*s, dtype=torch.bfloat16) * 0.8)(D, H * Dh) * (D ** -0.5)),
            "wo": _w((lambda *s: torch.randn(*s, dtype=torch.bfloat16) * 0.05)(H * Dh, D) * ((H * Dh) ** -0.5)),
        }
    _B_in = _B_setup()

    return {
        "A_x": _A_in["x"],
        "A_k_cache": _A_in["k_cache"],
        "A_v_cache": _A_in["v_cache"],
        "A_wq": _A_in["wq"],
        "A_wo": _A_in["wo"],
        "B_x": _B_in["x"],
        "B_k_cache": _B_in["k_cache"],
        "B_v_cache": _B_in["v_cache"],
        "B_wq": _B_in["wq"],
        "B_wo": _B_in["wo"],
    }

def reference(A_x, A_k_cache, A_v_cache, A_wq, A_wo, B_x, B_k_cache, B_v_cache, B_wq, B_wo):
    def _A_ref(x, k_cache, v_cache, wq, wo):
        B, Sq, D = x.shape
        H, Dh = 32, 128
        q = (lambda x, *d: x.permute(*d))((x @ wq).reshape(B, Sq, H, Dh), 0, 2, 1, 3)
        scores = torch.einsum("bhqd,bhkd->bhqk", q, k_cache) * (Dh ** -0.5)
        p = _softmax(scores, axis=-1)
        o = torch.einsum("bhqk,bhkd->bhqd", p, v_cache)
        return (lambda x, *d: x.permute(*d))(o, 0, 2, 1, 3).reshape(B, Sq, H * Dh) @ wo

    def _B_ref(x, k_cache, v_cache, wq, wo):
        B, Sq, D = x.shape
        H, Dh = 32, 128
        q = (lambda x, *d: x.permute(*d))((x @ wq).reshape(B, Sq, H, Dh), 0, 2, 1, 3)
        scores = torch.einsum("bhqd,bhkd->bhqk", q, k_cache) * (Dh ** -0.5)
        p = _softmax(scores, axis=-1)
        o = torch.einsum("bhqk,bhkd->bhqd", p, v_cache)
        return (lambda x, *d: x.permute(*d))(o, 0, 2, 1, 3).reshape(B, Sq, H * Dh) @ wo

    a = _A_ref(A_x, A_k_cache, A_v_cache, A_wq, A_wo)
    b = _B_ref(B_x, B_k_cache, B_v_cache, B_wq, B_wo)
    return torch.concatenate([a, b], axis=-1)
```

```cpp
#include <hip/hip_runtime.h>
#include <stdint.h>

typedef __bf16 bf16x8 __attribute__((ext_vector_type(8)));
typedef __bf16 bf16x4 __attribute__((ext_vector_type(4)));
typedef __bf16 bf16x2 __attribute__((ext_vector_type(2)));
typedef float f32x16 __attribute__((ext_vector_type(16)));
typedef float f32x4 __attribute__((ext_vector_type(4)));
typedef float f32x2 __attribute__((ext_vector_type(2)));
typedef unsigned u32x4 __attribute__((ext_vector_type(4)));
#define LAS __attribute__((address_space(3)))
#define DI __device__ __forceinline__

constexpr int NB = 8, SQ = 128, DM = 576, NH = 32, SK = 1024, DH = 128;
constexpr int KV_SLOT = 16384, NKV = 6, G_OFF = NKV * KV_SLOT, G_SLOT = 32768;
constexpr int LDS_BYTES = 163840;
constexpr float CSC = 0.12751743082f;
constexpr float THR_RAW = 8.0f / CSC;

struct Params {
  const void* x[2]; const void* k[2]; const void* v[2]; const void* wq[2]; const void* wo[2];
  void* o_ws; void* out;
};

DI unsigned cvtpk(float lo, float hi) { f32x2 v = {lo, hi}; bf16x2 b = __builtin_convertvector(v, bf16x2); return __builtin_bit_cast(unsigned, b); }
DI bf16x8 pack8(const f32x16& a, int base) {
  u32x4 w = {cvtpk(a[base + 0], a[base + 1]), cvtpk(a[base + 2], a[base + 3]), cvtpk(a[base + 4], a[base + 5]), cvtpk(a[base + 6], a[base + 7])};
  return __builtin_bit_cast(bf16x8, w);
}
DI float swap_max(float v) { auto r = __builtin_amdgcn_permlane32_swap(__float_as_uint(v), __float_as_uint(v), false, false); return fmaxf(__uint_as_float(r[0]), __uint_as_float(r[1])); }
DI float swap_sum(float v) { auto r = __builtin_amdgcn_permlane32_swap(__float_as_uint(v), __float_as_uint(v), false, false); return __uint_as_float(r[0]) + __uint_as_float(r[1]); }

template <bool NT> DI void dma4(unsigned m0v, unsigned v0, unsigned v1, unsigned v2, unsigned v3, __amdgpu_buffer_rsrc_t srd, unsigned soff) {
  if constexpr (NT)
    asm volatile("s_nop 4\n\ts_mov_b32 m0, %0\n\ts_nop 0\n\t"
                 "buffer_load_dwordx4 %1, %5, %6 offen nt lds\n\t"
                 "buffer_load_dwordx4 %2, %5, %6 offen offset:1024 nt lds\n\t"
                 "buffer_load_dwordx4 %3, %5, %6 offen offset:2048 nt lds\n\t"
                 "buffer_load_dwordx4 %4, %5, %6 offen offset:3072 nt lds"
                 :: "s"(m0v), "v"(v0), "v"(v1), "v"(v2), "v"(v3), "s"(srd), "s"(soff) : "memory");
  else
    asm volatile("s_nop 4\n\ts_mov_b32 m0, %0\n\ts_nop 0\n\t"
                 "buffer_load_dwordx4 %1, %5, %6 offen lds\n\t"
                 "buffer_load_dwordx4 %2, %5, %6 offen offset:1024 lds\n\t"
                 "buffer_load_dwordx4 %3, %5, %6 offen offset:2048 lds\n\t"
                 "buffer_load_dwordx4 %4, %5, %6 offen offset:3072 lds"
                 :: "s"(m0v), "v"(v0), "v"(v1), "v"(v2), "v"(v3), "s"(srd), "s"(soff) : "memory");
}
template <int N> DI void wait_vm() { asm volatile("s_waitcnt vmcnt(%0)" :: "i"(N) : "memory"); }
DI void bar() { asm volatile("s_waitcnt lgkmcnt(0)\n\ts_barrier" ::: "memory"); }
DI bf16x8 lds_b128(unsigned addr) { return *(const LAS bf16x8*)(uintptr_t)addr; }
DI bf16x4 lds_tr(unsigned addr) { return __builtin_amdgcn_ds_read_tr16_b64_v4bf16((LAS bf16x4*)(uintptr_t)addr); }
DI bf16x8 cat(bf16x4 a, bf16x4 b) { return __builtin_shufflevector(a, b, 0, 1, 2, 3, 4, 5, 6, 7); }
#define MFMA32(a, b, c) __builtin_amdgcn_mfma_f32_32x32x16_bf16((a), (b), (c), 0, 0, 0)

__global__ __launch_bounds__(512, 2) void attn_kernel(Params p) {
  extern __shared__ __attribute__((aligned(16))) char lds[];
  const unsigned ldsb = (unsigned)(uintptr_t)(LAS char*)lds;
  const int tid = threadIdx.x, lane = tid & 63;
  const int w = __builtin_amdgcn_readfirstlane(tid >> 6);
  const int qb = w & 3, team = w >> 2;
  const int r32 = lane & 31, hi = lane >> 5;
  const int wg = blockIdx.x, xcd = wg & 7, idx = wg >> 3;
  const int br = xcd >> 2, hq = xcd & 3, b = idx >> 2, hl = idx & 3;

  const __amdgpu_buffer_rsrc_t srdK = __builtin_amdgcn_make_buffer_rsrc((char*)p.k[br] - 4096, 0, NB * NH * SK * DH * 2 + 4096, 0x00020000);
  const __amdgpu_buffer_rsrc_t srdV = __builtin_amdgcn_make_buffer_rsrc((char*)p.v[br] - 4096, 0, NB * NH * SK * DH * 2 + 4096, 0x00020000);
  const __amdgpu_buffer_rsrc_t srdX = __builtin_amdgcn_make_buffer_rsrc((char*)p.x[br] - 4096, 0, NB * SQ * DM * 2 + 4096, 0x00020000);
  const __amdgpu_buffer_rsrc_t srdW = __builtin_amdgcn_make_buffer_rsrc((char*)p.wq[br] - 4096, 0, DM * NH * DH * 2 + 4096, 0x00020000);

  unsigned dv[8];
  {
    const unsigned RS = team == 0 ? 256u : 8192u;
#pragma unroll
    for (int i = 0; i < 4; ++i) {
      const unsigned rowl = 8 * (i >> 1) + ((lane >> 2) & 7);
      const unsigned ch = 8 * (i & 1) + 4 * (lane >> 5) + ((lane & 3) ^ ((2 * (i >> 1) + ((lane >> 4) & 1)) & 3));
      dv[i] = rowl * RS + ch * 16 + 4096u - 1024u * i;
      const unsigned xrow = 8 * i + ((lane >> 2) & 7);
      const unsigned xch = 4 * (lane >> 5) + ((lane & 3) ^ ((2 * i + ((lane >> 4) & 1)) & 3));
      dv[4 + i] = xrow * 1152u + xch * 16 + 4096u - 1024u * i;
    }
  }
  auto issue_kv = [&](int T) {
    const int r = T >> 4, t = T & 15;
    const unsigned head = 16 * r + 4 * hq + hl;
    const unsigned so = ((unsigned)(b * NH) + head) * (SK * DH * 2) + (unsigned)t * 16384u + (unsigned)qb * 4096u;
    const unsigned sk = (unsigned)((2 * T) % NKV), sv = (unsigned)((2 * T + 1) % NKV);
    dma4<true>(ldsb + sk * KV_SLOT + qb * 4096, dv[0], dv[1], dv[2], dv[3], srdK, so);
    dma4<true>(ldsb + sv * KV_SLOT + qb * 4096, dv[0], dv[1], dv[2], dv[3], srdV, so);
  };
  auto issue_q = [&](int r, int st) {
    const unsigned head = 16 * r + 4 * hq + hl;
    const unsigned gb = ldsb + G_OFF + (st & 1) * G_SLOT;
    dma4<false>(gb + qb * 4096, dv[4], dv[5], dv[6], dv[7], srdX, (unsigned)b * (SQ * DM * 2) + (unsigned)qb * (32 * 1152) + (unsigned)st * 128u);
    dma4<true>(gb + 16384 + qb * 4096, dv[0], dv[1], dv[2], dv[3], srdW, (unsigned)st * (64 * 8192) + (unsigned)qb * (16 * 8192) + head * 256u);
  };

  const int q4 = (lane & 15) >> 2, pp = lane & 3, blk = (lane >> 4) & 1;
  const int psw = ((pp & 1) << 1) | (pp >> 1);
  unsigned kro[2], xro[2], vtr[2], wtr[2];
#pragma unroll
  for (int j = 0; j < 2; ++j) {
    const unsigned krow = 32 * team + r32;
    kro[j] = 2048 * (krow >> 3) + 64 * (krow & 7) + 16 * ((2 * j + hi) ^ ((krow >> 2) & 3));
    const unsigned xrow = 32 * qb + r32;
    xro[j] = 1024 * (xrow >> 3) + 64 * (xrow & 7) + 16 * ((2 * j + hi) ^ ((xrow >> 2) & 3));
    vtr[j] = 2048 * (4 * team + j) + 64 * (4 * hi + q4) + 16 * ((2 * blk + (psw >> 1)) ^ ((2 * j + hi) & 3)) + 8 * (psw & 1);
    wtr[j] = 2048 * hi + 64 * (4 * j + q4) + 16 * ((2 * blk + (psw >> 1)) ^ ((2 * hi + j) & 3)) + 8 * (psw & 1) + 512 * (2 * team);
  }

  if (team == 1) { issue_q(0, 0); issue_q(0, 1); }
  else { issue_kv(0); issue_kv(1); issue_kv(2); }

  for (int r = 0; r < 2; ++r) {
    f32x16 qa[2] = {};
    for (int st = 0; st < 9; ++st) {
      if (team == 1) { if (st < 8) wait_vm<8>(); else wait_vm<0>(); }
      bar();
      const unsigned gb = ldsb + G_OFF + (st & 1) * G_SLOT;
#pragma unroll
      for (int ks = 0; ks < 4; ++ks) {
        const bf16x8 xf = lds_b128(gb + xro[ks & 1] + 512 * (ks >> 1));
#pragma unroll
        for (int c = 0; c < 2; ++c) {
          const bf16x8 wf = cat(lds_tr(gb + 16384 + wtr[0] + 4096 * ks + 512 * c), lds_tr(gb + 16384 + wtr[1] + 4096 * ks + 512 * c));
          qa[c] = MFMA32(wf, xf, qa[c]);
        }
      }
      bar();
      if (team == 1 && st + 2 < 9) issue_q(r, st + 2);
    }
    bf16x8 qf[8];
    {
      const unsigned gb = ldsb + G_OFF;
#pragma unroll
      for (int i = 0; i < 4; ++i) *(LAS bf16x8*)(uintptr_t)(gb + (w * 4 + i) * 1024 + lane * 16) = pack8(qa[i >> 1], 8 * (i & 1));
      bar();
#pragma unroll
      for (int s = 0; s < 8; ++s) qf[s] = lds_b128(gb + ((qb + 4 * (s >> 2)) * 4 + (s & 3)) * 1024 + lane * 16);
    }
    f32x16 o[4] = {};
    float m_ref = -1e30f, l_sum = 0.f;
    for (int t = 0; t < 16; ++t) {
      const int T = 16 * r + t;
      if (team == 0) { if (T == 0) wait_vm<16>(); else if (T < 31) wait_vm<8>(); else wait_vm<0>(); }
      bar();
      if (team == 0 && T >= 1 && T + 2 < 32) issue_kv(T + 2);
      const unsigned kb = ldsb + ((2 * T) % NKV) * KV_SLOT, vb = ldsb + ((2 * T + 1) % NKV) * KV_SLOT;
      f32x16 s = {};
#pragma unroll
      for (int ks = 0; ks < 8; ++ks) s = MFMA32(lds_b128(kb + kro[ks & 1] + 512 * (ks >> 1)), qf[ks], s);
      float mx = fmaxf(s[0], s[1]);
#pragma unroll
      for (int i = 2; i < 16; ++i) mx = fmaxf(mx, s[i]);
      mx = swap_max(mx);
      if (__any(mx > m_ref + THR_RAW)) {
        const float mn = fmaxf(m_ref, mx);
        const float al = __builtin_amdgcn_exp2f((m_ref - mn) * CSC);
        m_ref = mn; l_sum *= al;
#pragma unroll
        for (int c = 0; c < 4; ++c)
#pragma unroll
          for (int i = 0; i < 16; ++i) o[c][i] *= al;
      }
      const float nm = -m_ref * CSC;
      float ps = 0.f;
#pragma unroll
      for (int i = 0; i < 16; ++i) { s[i] = __builtin_amdgcn_exp2f(fmaf(s[i], CSC, nm)); ps += s[i]; }
      l_sum += ps;
      const bf16x8 p0 = pack8(s, 0), p1 = pack8(s, 8);
#pragma unroll
      for (int c = 0; c < 4; ++c) {
        o[c] = MFMA32(cat(lds_tr(vb + vtr[0] + 512 * c), lds_tr(vb + vtr[1] + 512 * c)), p0, o[c]);
        o[c] = MFMA32(cat(lds_tr(vb + vtr[0] + 4096 + 512 * c), lds_tr(vb + vtr[1] + 4096 + 512 * c)), p1, o[c]);
      }
    }
    {
      const unsigned gb = ldsb + G_OFF;
      l_sum = swap_sum(l_sum);
      bar();
      if (hi == 0) { *(LAS f32x2*)(uintptr_t)(gb + (w * 32 + r32) * 8) = f32x2{m_ref, l_sum}; }
      bar();
      const f32x2 ml = *(const LAS f32x2*)(uintptr_t)(gb + ((w ^ 4) * 32 + r32) * 8);
      const float M = fmaxf(m_ref, ml[0]);
      const float a0 = __builtin_amdgcn_exp2f((m_ref - M) * CSC), a1 = __builtin_amdgcn_exp2f((ml[0] - M) * CSC);
      const float sc = a0 / (l_sum * a0 + ml[1] * a1);
      bar();
#pragma unroll
      for (int i = 0; i < 2; ++i)
#pragma unroll
        for (int g = 0; g < 4; ++g) {
          const f32x16& src = team == 0 ? o[2 + i] : o[i];
          f32x4 v = {src[4 * g] * sc, src[4 * g + 1] * sc, src[4 * g + 2] * sc, src[4 * g + 3] * sc};
          *(LAS f32x4*)(uintptr_t)(gb + w * 8192 + (i * 4 + g) * 1024 + lane * 16) = v;
        }
      bar();
      const unsigned head = 16 * r + 4 * hq + hl;
      char* ob = (char*)p.o_ws + ((size_t)((br * NB + b) * NH + head) * 4 + qb) * 8192;
#pragma unroll
      for (int i = 0; i < 2; ++i) {
        f32x16 f;
#pragma unroll
        for (int g = 0; g < 4; ++g) {
          const f32x4 v = *(const LAS f32x4*)(uintptr_t)(gb + (w ^ 4) * 8192 + (i * 4 + g) * 1024 + lane * 16);
          const f32x16& own = team == 0 ? o[i] : o[2 + i];
#pragma unroll
          for (int e = 0; e < 4; ++e) f[4 * g + e] = own[4 * g + e] * sc + v[e];
        }
        const int c = 2 * team + i;
        *(bf16x8*)(ob + (2 * c) * 1024 + lane * 16) = pack8(f, 0);
        *(bf16x8*)(ob + (2 * c + 1) * 1024 + lane * 16) = pack8(f, 8);
      }
      bar();
      if (r == 0 && team == 1) { issue_q(1, 0); issue_q(1, 1); }
    }
  }
}

__global__ __launch_bounds__(64) void outproj_naive(Params p) {
  const int lane = threadIdx.x, r32 = lane & 31, hi = lane >> 5;
  const int rb = blockIdx.x;
  const int cb = blockIdx.y;
  const int br = rb >> 5, b = (rb >> 2) & 7, qb = rb & 3;
  const unsigned short* wo = (const unsigned short*)p.wo[br];
  f32x16 acc = {};
  for (int h = 0; h < NH; ++h) {
    const char* ob = (const char*)p.o_ws + ((size_t)((br * NB + b) * NH + h) * 4 + qb) * 8192;
    for (int s = 0; s < 8; ++s) {
      const bf16x8 a = *(const bf16x8*)(ob + s * 1024 + lane * 16);
      typedef unsigned short u16x8 __attribute__((ext_vector_type(8)));
      u16x8 bv;
      for (int j = 0; j < 8; ++j) bv[j] = wo[(size_t)(h * 128 + 16 * s + 8 * hi + j) * DM + cb * 32 + r32];
      acc = MFMA32(a, __builtin_bit_cast(bf16x8, bv), acc);
    }
  }
  __bf16* out = (__bf16*)p.out;
  for (int i = 0; i < 16; ++i) {
    const int row = b * SQ + qb * 32 + (i & 3) + 8 * (i >> 2) + 4 * hi;
    out[(size_t)row * 1152 + br * 576 + cb * 32 + r32] = (__bf16)acc[i];
  }
}

extern "C" void kernel_launch(void* const* d_in, const int* in_sizes, int n_in, void* d_out, int out_size, void* d_ws, size_t ws_size, hipStream_t stream) {
  Params p;
  for (int i = 0; i < 2; ++i) { p.x[i] = d_in[5 * i + 0]; p.k[i] = d_in[5 * i + 1]; p.v[i] = d_in[5 * i + 2]; p.wq[i] = d_in[5 * i + 3]; p.wo[i] = d_in[5 * i + 4]; }
  p.o_ws = d_ws; p.out = d_out;
  (void)hipFuncSetAttribute((const void*)attn_kernel, hipFuncAttributeMaxDynamicSharedMemorySize, LDS_BYTES);
  hipLaunchKernelGGL(attn_kernel, dim3(256), dim3(512), LDS_BYTES, stream, p);
  hipLaunchKernelGGL(outproj_naive, dim3(64, 18), dim3(64), 0, stream, p);
}
```

```cpp
#include <hip/hip_runtime.h>
#include <stdint.h>

typedef __bf16 bf16x8 __attribute__((ext_vector_type(8)));
typedef __bf16 bf16x4 __attribute__((ext_vector_type(4)));
typedef __bf16 bf16x2 __attribute__((ext_vector_type(2)));
typedef float f32x16 __attribute__((ext_vector_type(16)));
typedef float f32x4 __attribute__((ext_vector_type(4)));
typedef float f32x2 __attribute__((ext_vector_type(2)));
typedef unsigned u32x4 __attribute__((ext_vector_type(4)));
#define LAS __attribute__((address_space(3)))
#define DI __device__ __forceinline__
#define SB() __builtin_amdgcn_sched_barrier(0)
#define PIN(x) asm volatile("" :: "v"(x))

constexpr bool XNOMAX = false;
#define MAPV 1
#define WQNT false
constexpr int NB = 8, SQ = 128, DM = 576, NH = 32, SK = 1024, DH = 128;
constexpr int G_OFF = 98304, G_SLOT = 32768;
constexpr int LDS_BYTES = 163840;
constexpr float CSC = 0.12751743082f;
constexpr float THR = 8.0f;

struct Params {
  const void* x[2]; const void* k[2]; const void* v[2]; const void* wq[2]; const void* wo[2];
  void* o_ws; void* out;
};

DI unsigned cvtpk(float lo, float hi) { f32x2 v = {lo, hi}; bf16x2 b = __builtin_convertvector(v, bf16x2); return __builtin_bit_cast(unsigned, b); }
DI bf16x8 pack8(const f32x16& a, int base) {
  u32x4 w = {cvtpk(a[base + 0], a[base + 1]), cvtpk(a[base + 2], a[base + 3]), cvtpk(a[base + 4], a[base + 5]), cvtpk(a[base + 6], a[base + 7])};
  return __builtin_bit_cast(bf16x8, w);
}
DI bf16x8 pack8s(const f32x16& a, int base, float s) {
  u32x4 w = {cvtpk(a[base + 0] * s, a[base + 1] * s), cvtpk(a[base + 2] * s, a[base + 3] * s), cvtpk(a[base + 4] * s, a[base + 5] * s), cvtpk(a[base + 6] * s, a[base + 7] * s)};
  return __builtin_bit_cast(bf16x8, w);
}
DI float fadd_s(float a, float b) { float r; asm("v_add_f32_e32 %0, %1, %2" : "=v"(r) : "v"(a), "v"(b)); return r; }
DI float fsub_s(float a, float b) { float r; asm("v_sub_f32_e32 %0, %1, %2" : "=v"(r) : "v"(a), "v"(b)); return r; }
DI float max2_s(float a, float b) { return __builtin_elementwise_maximum(a, b); }
DI float max3_s(float a, float b, float c) { return max2_s(max2_s(a, b), c); }
DI float swap_max(float v) { auto r = __builtin_amdgcn_permlane32_swap(__float_as_uint(v), __float_as_uint(v), false, false); return max2_s(__uint_as_float(r[0]), __uint_as_float(r[1])); }
DI float swap_sum(float v) { auto r = __builtin_amdgcn_permlane32_swap(__float_as_uint(v), __float_as_uint(v), false, false); return __uint_as_float(r[0]) + __uint_as_float(r[1]); }
DI int opaque_lane() { int l = threadIdx.x; asm volatile("" : "+v"(l)); return l & 63; }

template <bool NT> DI void dma1(unsigned m0v, unsigned v, __amdgpu_buffer_rsrc_t srd, unsigned s) {
  if constexpr (NT) asm volatile("s_mov_b32 m0, %0\n\ts_nop 0\n\tbuffer_load_dwordx4 %1, %2, %3 offen nt lds" :: "s"(m0v), "v"(v), "s"(srd), "s"(s) : "memory");
  else asm volatile("s_mov_b32 m0, %0\n\ts_nop 0\n\tbuffer_load_dwordx4 %1, %2, %3 offen lds" :: "s"(m0v), "v"(v), "s"(srd), "s"(s) : "memory");
}
template <int N> DI void wait_vm() { asm volatile("s_waitcnt vmcnt(%0)" :: "i"(N) : "memory"); }
DI void bar() { asm volatile("s_waitcnt lgkmcnt(0)\n\ts_barrier" ::: "memory"); }
DI void barX() { bar(); }
DI void barY() { bar(); }
DI bf16x8 lds_b128(unsigned addr) { return *(const LAS bf16x8*)(uintptr_t)addr; }
DI bf16x4 lds_tr(unsigned addr) { return __builtin_amdgcn_ds_read_tr16_b64_v4bf16((LAS bf16x4*)(uintptr_t)addr); }
DI bf16x8 cat(bf16x4 a, bf16x4 b) { return __builtin_shufflevector(a, b, 0, 1, 2, 3, 4, 5, 6, 7); }
#define MFMA32(a, b, c) __builtin_amdgcn_mfma_f32_32x32x16_bf16((a), (b), (c), 0, 0, 0)

#define STAMP(i)
#define TSTAMP(T, k)

struct Ctx {
  unsigned dva, dvb;
  unsigned dxa, dxb;
  unsigned kro[2], vtr[2];
  __amdgpu_buffer_rsrc_t srdK, srdV, srdX, srdW;
  unsigned kv_s, x_s, w_s;
  int w;
};
template <bool NT> DI void dma_tile(const Ctx& c, int i, unsigned lds_tile, __amdgpu_buffer_rsrc_t srd, unsigned so) {
  dma1<NT>(lds_tile + (c.w & 3) * 4096 + i * 1024, (i & 2) ? c.dvb : c.dva, srd, so + (i & 1) * 128u);
}
DI void issue_qp(const Ctx& c, int r, int st, int i, unsigned gb) {
  if (i < 4) dma1<false>(gb + (c.w & 3) * 4096 + i * 1024, (i & 1) ? c.dxb : c.dxa, c.srdX, c.x_s + (unsigned)st * 128u + (unsigned)i * (8 * 1152));
  else dma_tile<WQNT>(c, i - 4, gb + 16384, c.srdW, c.w_s + (unsigned)st * (64 * 8192) + (unsigned)r * (16 * 256));
}
struct QAddr { unsigned x0, x1, w0, w1; };
DI QAddr q_addr(const Ctx& c, unsigned gb) {
  const int lane = opaque_lane();
  const int r32 = lane & 31, hi = lane >> 5, q4 = (lane & 15) >> 2, pp = lane & 3, blk = (lane >> 4) & 1;
  const int psw = ((pp & 1) << 1) | (pp >> 1);
  const unsigned xrow = 32 * (c.w & 3) + r32;
  const unsigned xb = gb + 1024 * (xrow >> 3) + 64 * (xrow & 7);
  QAddr a;
  a.x0 = xb + 16 * (hi ^ ((xrow >> 2) & 3)); a.x1 = xb + 16 * ((2 + hi) ^ ((xrow >> 2) & 3));
  const unsigned wb = gb + 16384 + 2048 * hi + 64 * q4 + 8 * (psw & 1) + 1024 * (c.w >> 2);
  a.w0 = wb + 16 * ((2 * blk + (psw >> 1)) ^ ((2 * hi) & 3)); a.w1 = wb + 256 + 16 * ((2 * blk + (psw >> 1)) ^ ((2 * hi + 1) & 3));
  asm volatile("" : "+v"(a.x0), "+v"(a.x1), "+v"(a.w0), "+v"(a.w1));
  return a;
}
DI void q_stage(const Ctx& c, unsigned gb, f32x16 (&qa)[2]) {
  const QAddr a = q_addr(c, gb);
#pragma unroll
  for (int hf = 0; hf < 2; ++hf) {
    bf16x8 xf[2]; bf16x4 wl[2][2], wh[2][2];
#pragma unroll
    for (int k2 = 0; k2 < 2; ++k2) {
      const int ks = 2 * hf + k2;
      xf[k2] = lds_b128(((ks & 1) ? a.x1 : a.x0) + 512 * (ks >> 1));
#pragma unroll
      for (int cc = 0; cc < 2; ++cc) { wl[k2][cc] = lds_tr(a.w0 + 4096 * ks + 512 * cc); wh[k2][cc] = lds_tr(a.w1 + 4096 * ks + 512 * cc); }
    }
    SB();
#pragma unroll
    for (int k2 = 0; k2 < 2; ++k2)
#pragma unroll
      for (int cc = 0; cc < 2; ++cc) qa[cc] = MFMA32(cat(wl[k2][cc], wh[k2][cc]), xf[k2], qa[cc]);
    SB();
  }
}
struct QFrag { bf16x8 xf[4]; bf16x4 wl[4][2], wh[4][2]; };
template <int KS> DI void q_read_ks(const QAddr& a, QFrag& f) {
  f.xf[KS] = lds_b128(((KS & 1) ? a.x1 : a.x0) + 512 * (KS >> 1));
#pragma unroll
  for (int cc = 0; cc < 2; ++cc) { f.wl[KS][cc] = lds_tr(a.w0 + 4096 * KS + 512 * cc); f.wh[KS][cc] = lds_tr(a.w1 + 4096 * KS + 512 * cc); }
}
template <bool RD, class H> DI void q_pipe(const QAddr& an, QFrag& cur, QFrag& nxt, f32x16 (&qa)[2], H&& hook) {
#define QP(KS) \
  qa[0] = MFMA32(cat(cur.wl[KS][0], cur.wh[KS][0]), cur.xf[KS], qa[0]); \
  if constexpr (RD) q_read_ks<KS>(an, nxt); \
  hook(2 * KS); \
  SB(); \
  qa[1] = MFMA32(cat(cur.wl[KS][1], cur.wh[KS][1]), cur.xf[KS], qa[1]); \
  hook(2 * KS + 1); \
  SB();
  QP(0) QP(1) QP(2) QP(3)
#undef QP
}

struct AState {
  f32x16 o[4];
  f32x16 sa, sb;
  bf16x8 kf[8];
  bf16x4 vl[8], vh[8];
  u32x4 p0, p1;
  float m_ref, l0, l1, f;
  bool pend;
};
template <int U> DI void sm_slot(AState& a, f32x16& s) {
  if constexpr (U < 16) { s[U] = __builtin_amdgcn_exp2f(s[U]); PIN(s[U]); }
  if constexpr (U + 1 < 16) { s[U + 1] = fsub_s(s[U + 1], a.m_ref); PIN(s[U + 1]); }
  if constexpr (U >= 1 && U <= 16) {
    constexpr int E = U - 1;
    if (E & 1) a.l1 = fadd_s(a.l1, s[E]); else a.l0 = fadd_s(a.l0, s[E]);
    if constexpr (E & 1) {
      if constexpr (E < 8) { a.p0[E >> 1] = cvtpk(s[E - 1], s[E]); PIN(a.p0[E >> 1]); PIN(a.l1); }
      else { a.p1[(E - 8) >> 1] = cvtpk(s[E - 1], s[E]); PIN(a.p1[(E - 8) >> 1]); PIN(a.l1); }
    } else PIN(a.l0);
  }
}
template <bool QK, bool FIN, class H> DI void phaseA(const Ctx& c, AState& a, f32x16& sn, f32x16& sp, const bf16x8 (&qf)[8], unsigned vb, H&& hook) {
  unsigned v0 = vb + c.vtr[0], v1 = vb + c.vtr[1];
  asm volatile("" : "+v"(v0), "+v"(v1));
#define VRD(f) do { a.vl[f] = lds_tr(v0 + 4096 * ((f) & 1) + 512 * ((f) >> 1)); a.vh[f] = lds_tr(v1 + 4096 * ((f) & 1) + 512 * ((f) >> 1)); } while (0)
#define GA(i) do { \
    if constexpr (QK) { if constexpr (i == 0) sn = MFMA32(a.kf[0], qf[0], f32x16{}); else sn = MFMA32(a.kf[i], qf[i], sn); } \
    if constexpr (FIN) { \
      if constexpr (i == 0) { VRD(0); } if constexpr (i == 1) { VRD(2); VRD(4); } if constexpr (i == 2) { VRD(6); } if constexpr (i == 3) { VRD(1); VRD(3); } if constexpr (i == 4) { VRD(5); } if constexpr (i == 5) { VRD(7); } \
      sm_slot<4 + i>(a, sp); } \
    hook(i); SB(); } while (0)
  GA(0); GA(1); GA(2); GA(3); GA(4); GA(5); GA(6); GA(7);
#undef GA
#undef VRD
}
template <bool PV, bool START, bool KR, class H> DI void phaseB(const Ctx& c, AState& a, f32x16& sn, f32x16& sp, unsigned kb, H&& hook) {
  unsigned k0 = kb + c.kro[0], k1 = kb + c.kro[1];
  asm volatile("" : "+v"(k0), "+v"(k1));
  float mx = 0.f, mxa = 0.f, mxb = 0.f;
#define KRD(f) a.kf[f] = lds_b128((((f) & 1) ? k1 : k0) + 512 * ((f) >> 1))
#define GB(i) do { \
    if constexpr (PV && i == 4) sm_slot<16>(a, sp);   \
    if constexpr (PV) { constexpr int f = 2 * (i & 3) + (i >> 2); a.o[i & 3] = MFMA32(cat(a.vl[f], a.vh[f]), __builtin_bit_cast(bf16x8, (i < 4) ? a.p0 : a.p1), a.o[i & 3]); } \
    if constexpr (KR) { if constexpr (i == 0) { KRD(0); } if constexpr (i == 1) { KRD(1); } if constexpr (i == 2) { KRD(2); KRD(3); } if constexpr (i == 3) { KRD(4); } if constexpr (i == 4) { KRD(5); KRD(6); } if constexpr (i == 5) { KRD(7); } } \
    if constexpr (PV && i < 4) sm_slot<12 + (i & 3)>(a, sp); \
    if constexpr (START && !XNOMAX) { \
      if constexpr (i == 1) { mxa = max3_s(sn[0], sn[1], sn[2]); mxb = max3_s(sn[3], sn[4], sn[5]); mxa = max3_s(mxa, sn[6], sn[7]); mxb = max3_s(mxb, sn[8], sn[9]); PIN(mxa); PIN(mxb); } \
      if constexpr (i == 2) { mxa = max3_s(mxa, sn[10], sn[11]); mxb = max3_s(mxb, sn[12], sn[13]); mx = max3_s(mxa, sn[14], sn[15]); mx = max2_s(mx, mxb); mx = swap_max(mx); PIN(mx); } \
      if constexpr (i == 4) { \
        if (!PV || __any(mx > a.m_ref + THR)) { \
          const float mn = PV ? max2_s(mx, a.m_ref) : mx; \
          a.f = __builtin_amdgcn_exp2f(a.m_ref - mn); \
          a.m_ref = mn; a.l0 *= a.f; a.l1 *= a.f; a.pend = true; \
        } \
        sn[0] = fsub_s(sn[0], a.m_ref); \
      } \
    } \
    if constexpr (START) { if constexpr (i >= 4) sm_slot<(i & 3)>(a, sn); } \
    hook(i); SB(); } while (0)
  GB(0); GB(1); GB(2); GB(3); GB(4); GB(5); GB(6); GB(7);
#undef GB
#undef KRD
  if constexpr (START || PV) { PIN(a.l0); PIN(a.l1); }
  if constexpr (START && PV) {
    if (a.pend) {
#pragma unroll
      for (int cc = 0; cc < 4; ++cc)
#pragma unroll
        for (int e = 0; e < 16; ++e) a.o[cc][e] *= a.f;
    }
  }
  a.pend = false;
}

template <int TEAM> DI void body(const Params& p, char* lds) {
  const int tid = threadIdx.x, lane = tid & 63;
  const int w = __builtin_amdgcn_readfirstlane(tid >> 6);
  const int qb = w & 3;
  constexpr int team = TEAM;
  const int wg = blockIdx.x, xcd = wg & 7, idx = wg >> 3;
  const int br = xcd >> 2, b = 2 * (xcd & 3) + (idx >> 4), hl = idx & 15;
  const unsigned head0 = hl;
  const unsigned ldsb = (unsigned)(uintptr_t)(LAS char*)lds;

  Ctx c;
  c.w = w;
  {
    const int r32 = lane & 31, hi = lane >> 5;
    const unsigned r7 = (lane >> 2) & 7, bit = (lane >> 4) & 1, h4 = 4 * (lane >> 5), l3 = lane & 3;
    if constexpr (team == 0) {
      c.srdK = __builtin_amdgcn_make_buffer_rsrc((void*)p.k[br], 0, NB * NH * SK * DH * 2, 0x00020000);
      c.srdV = __builtin_amdgcn_make_buffer_rsrc((void*)p.v[br], 0, NB * NH * SK * DH * 2, 0x00020000);
      c.dva = r7 * 256u + (h4 + (l3 ^ bit)) * 16;
      c.dvb = (r7 + 8) * 256u + (h4 + (l3 ^ (2 + bit))) * 16;
      c.kv_s = ((unsigned)(b * NH) + head0) * (SK * DH * 2) + (unsigned)qb * (16 * 256);
    } else {
      c.srdX = __builtin_amdgcn_make_buffer_rsrc((void*)p.x[br], 0, NB * SQ * DM * 2, 0x00020000);
      c.srdW = __builtin_amdgcn_make_buffer_rsrc((void*)p.wq[br], 0, DM * NH * DH * 2, 0x00020000);
      c.dva = r7 * 8192u + (h4 + (l3 ^ bit)) * 16;
      c.dvb = (r7 + 8) * 8192u + (h4 + (l3 ^ (2 + bit))) * 16;
      c.dxa = r7 * 1152u + (h4 + (l3 ^ bit)) * 16;
      c.dxb = r7 * 1152u + (h4 + (l3 ^ (2 + bit))) * 16;
      c.x_s = (unsigned)b * (SQ * DM * 2) + (unsigned)qb * (32 * 1152);
      c.w_s = (unsigned)qb * (16 * 8192) + head0 * 256u;
    }
    const int q4 = (lane & 15) >> 2, pp = lane & 3, blk = (lane >> 4) & 1;
    const int psw = ((pp & 1) << 1) | (pp >> 1);
#pragma unroll
    for (int j = 0; j < 2; ++j) {
      const unsigned krow = 32 * team + r32;
      c.kro[j] = 2048 * (krow >> 3) + 64 * (krow & 7) + 16 * ((2 * j + hi) ^ ((krow >> 2) & 3));
      c.vtr[j] = 2048 * (4 * team + j) + 64 * (4 * hi + q4) + 16 * ((2 * blk + (psw >> 1)) ^ ((2 * j + hi) & 3)) + 8 * (psw & 1);
    }
  }
  const unsigned G0 = ldsb + G_OFF, G1 = G0 + G_SLOT;
  const unsigned R0 = ldsb, R1 = R0 + 32768, R2 = R1 + 32768, GX = R2;

  auto q_exchange = [&](f32x16 (&qa)[2], bf16x8 (&qf)[8], unsigned gb) {
    const int lane = opaque_lane(); asm volatile("" : "+s"(gb));
#pragma unroll
    for (int i = 0; i < 4; ++i) {
      f32x16 t;
#pragma unroll
      for (int e = 0; e < 8; ++e) t[e] = (float)(__bf16)qa[i >> 1][8 * (i & 1) + e];
      *(LAS bf16x8*)(uintptr_t)(gb + (w * 4 + i) * 1024 + lane * 16) = pack8s(t, 0, CSC);
    }
    bar();
#pragma unroll
    for (int s = 0; s < 8; ++s) qf[s] = lds_b128(gb + ((qb + 4 * (s >> 2)) * 4 + (s & 3)) * 1024 + lane * 16);
  };
  auto merge_store = [&](int r, f32x16 (&o)[4], float m_ref, float l_sum, unsigned gb) {
    const int lane = opaque_lane(); asm volatile("" : "+s"(gb));
    const int r32 = lane & 31, hi = lane >> 5;
    l_sum = swap_sum(l_sum);
    if (hi == 0) { *(LAS f32x2*)(uintptr_t)(gb + (w * 32 + r32) * 8) = f32x2{m_ref, l_sum}; }
    bar();
    const f32x2 ml = *(const LAS f32x2*)(uintptr_t)(gb + ((w ^ 4) * 32 + r32) * 8);
    const float M = fmaxf(m_ref, ml[0]);
    const float a0 = __builtin_amdgcn_exp2f(m_ref - M), a1 = __builtin_amdgcn_exp2f(ml[0] - M);
    const float sc = a0 / (l_sum * a0 + ml[1] * a1);
    bar();
#pragma unroll
    for (int i = 0; i < 2; ++i)
#pragma unroll
      for (int g = 0; g < 4; ++g) { const f32x16& s = o[(team ? 0 : 2) + i]; *(LAS f32x4*)(uintptr_t)(gb + w * 8192 + (i * 4 + g) * 1024 + lane * 16) = f32x4{s[4 * g] * sc, s[4 * g + 1] * sc, s[4 * g + 2] * sc, s[4 * g + 3] * sc}; }
    bar();
    char* ob = (char*)p.o_ws + ((size_t)((br * NB + b) * NH + 16 * r + head0) * 4 + qb) * 8192;
#pragma unroll
    for (int i = 0; i < 2; ++i) {
      const int cc = 2 * team + i;
      f32x16 f;
#pragma unroll
      for (int g = 0; g < 4; ++g) {
        const f32x4 v = *(const LAS f32x4*)(uintptr_t)(gb + (w ^ 4) * 8192 + (i * 4 + g) * 1024 + lane * 16);
#pragma unroll
        for (int e = 0; e < 4; ++e) f[4 * g + e] = o[cc][4 * g + e] * sc + v[e];
      }
      *(bf16x8*)(ob + (2 * cc) * 1024 + lane * 16) = pack8(f, 0);
      *(bf16x8*)(ob + (2 * cc + 1) * 1024 + lane * 16) = pack8(f, 8);
    }
  };

  STAMP(0);
  auto tile_off = [&](int T) -> unsigned { return c.kv_s + (unsigned)(T >> 4) * (16u * SK * DH * 2) + (unsigned)(T & 15) * 16384u; };
  auto issue_V = [&](int T, unsigned sl, int i) {
    if (T <= 31) dma_tile<true>(c, i, sl, c.srdV, tile_off(T));
  };
  auto issue_K = [&](int T, unsigned sl, int i) {
    if (T <= 31) dma_tile<true>(c, i, sl, c.srdK, tile_off(T));
  };
  auto wait_tail = [&](int n) { if (n >= 12) wait_vm<12>(); else if (n >= 8) wait_vm<8>(); else if (n >= 4) wait_vm<4>(); else wait_vm<0>(); };
  if constexpr (team == 0) {
    const unsigned r7 = (lane >> 2) & 7, bit = (lane >> 4) & 1, h4 = 4 * (lane >> 5), l3 = lane & 3;
    c.srdX = __builtin_amdgcn_make_buffer_rsrc((void*)p.x[br], 0, NB * SQ * DM * 2, 0x00020000);
    c.dxa = r7 * 1152u + (h4 + (l3 ^ bit)) * 16;
    c.dxb = r7 * 1152u + (h4 + (l3 ^ (2 + bit))) * 16;
    c.x_s = (unsigned)b * (SQ * DM * 2) + (unsigned)qb * (32 * 1152);
  }
  auto p0_issue = [&](int st, int i, unsigned gb) { issue_qp(c, 0, st, team == 0 ? i : 4 + i, gb); };
#pragma unroll
  for (int i = 0; i < 4; ++i) p0_issue(0, i, GX);
#pragma unroll
  for (int i = 0; i < 4; ++i) p0_issue(1, i, G0);
#pragma unroll
  for (int i = 0; i < 4; ++i) p0_issue(2, i, G1);
  if constexpr (team == 0) {
#pragma unroll
    for (int i = 0; i < 4; ++i) issue_K(0, R0, i);
#pragma unroll
    for (int i = 0; i < 4; ++i) issue_K(1, R0 + 16384, i);
#pragma unroll
    for (int i = 0; i < 4; ++i) issue_V(0, R1, i);
#pragma unroll
    for (int i = 0; i < 4; ++i) issue_K(2, R1 + 16384, i);
  }
  bf16x8 qf[8];
  f32x16 qa[2] = {};
  {
    QFrag fa, fb;
    const QAddr a0 = q_addr(c, 0);
    auto qaddr = [&](unsigned gb) { QAddr a; a.x0 = a0.x0 + gb; a.x1 = a0.x1 + gb; a.w0 = a0.w0 + gb; a.w1 = a0.w1 + gb; asm volatile("" : "+v"(a.x0), "+v"(a.x1), "+v"(a.w0), "+v"(a.w1)); return a; };
    unsigned gcur = GX, gnxt = G0, gnn = G1;
    auto grot = [&]() { const unsigned tmp = gcur; gcur = gnxt; gnxt = gnn; gnn = tmp; };
#define KVW(n) (n)
    if constexpr (team == 1) wait_vm<8>(); else wait_vm<8 + KVW(16)>();
    bar();
    STAMP(8);
    { const QAddr an = qaddr(gcur); q_read_ks<0>(an, fa); q_read_ks<1>(an, fa); q_read_ks<2>(an, fa); q_read_ks<3>(an, fa); }
#define QIT(g, CUR, NXT) do { \
      if constexpr (team == 1) { if ((g) < 7) wait_vm<4>(); else wait_vm<0>(); } \
      else { if ((g) < 2) wait_vm<4 + KVW(16)>(); else if ((g) < 7) wait_vm<4>(); else wait_vm<KVW(8)>(); } \
      bar(); \
      const QAddr an = qaddr(gnxt); \
      q_pipe<true>(an, CUR, NXT, qa, [&](int i) { \
        if ((i & 1) == team) { if ((g) + 3 < 9) p0_issue((g) + 3, i >> 1, gcur); } \
        if constexpr (team == 0) { if ((g) == 6) { if (i < 4) issue_V(1, R2, i); else issue_K(3, R2 + 16384, i - 4); } } }); \
      grot(); } while (0)
#pragma unroll 1
    for (int g = 0; g < 8; g += 2) { QIT(g, fa, fb); QIT(g + 1, fb, fa); }
#undef QIT
    { const QAddr an = qaddr(gnxt); q_pipe<false>(an, fa, fb, qa, [&](int) {}); }
  }
  STAMP(1);
  q_exchange(qa, qf, G0);
  qa[0] = f32x16{}; qa[1] = f32x16{};
  STAMP(2);
  AState a;
  {
    unsigned k0 = R0 + c.kro[0], k1 = R0 + c.kro[1];
    asm volatile("" : "+v"(k0), "+v"(k1));
#pragma unroll
    for (int i = 0; i < 8; ++i) a.kf[i] = lds_b128(((i & 1) ? k1 : k0) + 512 * (i >> 1));
  }
  unsigned pcur = R0, pnxt = R1, pnn = R2;
  auto rot = [&]() { const unsigned tmp = pcur; pcur = pnxt; pnxt = pnn; pnn = tmp; };
  auto nohook = [&](int) {};
#pragma unroll 1
  for (int r = 0; r < 2; ++r) {
    const int T0 = 16 * r;
#pragma unroll
    for (int cc = 0; cc < 4; ++cc) a.o[cc] = f32x16{};
    a.m_ref = 0.f; a.l0 = 0.f; a.l1 = 0.f; a.f = 1.f; a.pend = false;
    phaseA<true, false>(c, a, a.sa, a.sb, qf, 0, nohook);
    if (r == 0) bar();
    phaseB<false, true, true>(c, a, a.sa, a.sb, pcur + 16384, [&](int i) { if constexpr (team == 0) { if ((i & 1) == 0 && r == 0) issue_V(2, pcur, i >> 1); } });
    rot();
#define STEP(t_, SN, SP) do { const int t = (t_), T = T0 + t; \
      const bool gi = (r == 0) && t >= 2 && t <= 10, gc = (r == 0) && t >= 3 && t <= 11; \
      TSTAMP(T, 0); \
      if constexpr (team == 0) { if (T <= 29) wait_vm<16>(); else wait_tail(T == 30 ? 12 : 4); } \
      barX(); \
      TSTAMP(T, 1); \
      phaseA<true, true>(c, a, SN, SP, qf, pcur, [&](int i) { \
        if constexpr (team == 0) { if ((i & 1) == 0) issue_K(T + 3, pnn + 16384, i >> 1); } \
        else { if (gi) issue_qp(c, 1, t - 2, i, (t & 1) ? G1 : G0); } }); \
      TSTAMP(T, 2); \
      if constexpr (team == 0) { if (T <= 28) wait_vm<16>(); else wait_tail(T == 29 ? 12 : (T == 30 ? 8 : 4)); } \
      else { if (gc) { if (gi) wait_vm<8>(); else wait_vm<0>(); } } \
      barY(); \
      TSTAMP(T, 3); \
      phaseB<true, true, true>(c, a, SN, SP, pcur + 16384, [&](int i) { if constexpr (team == 0) { if ((i & 1) == 0) issue_V(T + 2, pcur, i >> 1); } }); \
      if (gc) q_stage(c, ((t - 1) & 1) ? G1 : G0, qa); \
      rot(); \
    } while (0)
#pragma unroll 1
    for (int tt = 1; tt < 15; tt += 2) { STEP(tt, a.sb, a.sa); STEP(tt + 1, a.sa, a.sb); }
    STEP(15, a.sb, a.sa);
    {
      const int T = T0 + 16;
      if constexpr (team == 0) { if (r == 0) wait_vm<16>(); else wait_vm<0>(); }
      bar();
      phaseA<false, true>(c, a, a.sa, a.sb, qf, pcur, [&](int i) { if constexpr (team == 0) { if ((i & 1) == 0 && r == 0) issue_K(T + 3, pnn + 16384, i >> 1); } });
      if constexpr (team == 0) { if (r == 0) wait_vm<16>(); }
      bar();
      phaseB<true, false, false>(c, a, a.sa, a.sb, 0, [&](int i) { if constexpr (team == 0) { if ((i & 1) == 0 && r == 0) issue_V(T + 2, pcur, i >> 1); } });
    }
    STAMP(3 + 3 * r);
    bar();
    merge_store(r, a.o, a.m_ref, a.l0 + a.l1, G0);
    STAMP(4 + 3 * r);
    if (r == 0) { bar(); q_exchange(qa, qf, G0); STAMP(5); }
  }
}

__global__ __launch_bounds__(512, 2) void attn_kernel(Params p) {
  extern __shared__ __attribute__((aligned(16))) char lds[];
  if (__builtin_amdgcn_readfirstlane(threadIdx.x >> 8) == 0) body<0>(p, lds); else body<1>(p, lds);
}

__global__ __launch_bounds__(64) void outproj_naive(Params p) {
  const int lane = threadIdx.x, r32 = lane & 31, hi = lane >> 5;
  const int rb = blockIdx.x;
  const int cb = blockIdx.y;
  const int br = rb >> 5, b = (rb >> 2) & 7, qb = rb & 3;
  const unsigned short* wo = (const unsigned short*)p.wo[br];
  f32x16 acc = {};
  for (int h = 0; h < NH; ++h) {
    const char* ob = (const char*)p.o_ws + ((size_t)((br * NB + b) * NH + h) * 4 + qb) * 8192;
    for (int s = 0; s < 8; ++s) {
      const bf16x8 a = *(const bf16x8*)(ob + s * 1024 + lane * 16);
      typedef unsigned short u16x8 __attribute__((ext_vector_type(8)));
      u16x8 bv;
      for (int j = 0; j < 8; ++j) bv[j] = wo[(size_t)(h * 128 + 16 * s + 8 * hi + j) * DM + cb * 32 + r32];
      acc = MFMA32(a, __builtin_bit_cast(bf16x8, bv), acc);
    }
  }
  __bf16* out = (__bf16*)p.out;
  for (int i = 0; i < 16; ++i) {
    const int row = b * SQ + qb * 32 + (i & 3) + 8 * (i >> 2) + 4 * hi;
    out[(size_t)row * 1152 + br * 576 + cb * 32 + r32] = (__bf16)acc[i];
  }
}

extern "C" void kernel_launch(void* const* d_in, const int* in_sizes, int n_in, void* d_out, int out_size, void* d_ws, size_t ws_size, hipStream_t stream) {
  Params p;
  for (int i = 0; i < 2; ++i) { p.x[i] = d_in[5 * i + 0]; p.k[i] = d_in[5 * i + 1]; p.v[i] = d_in[5 * i + 2]; p.wq[i] = d_in[5 * i + 3]; p.wo[i] = d_in[5 * i + 4]; }
  p.o_ws = d_ws; p.out = d_out;
  (void)hipFuncSetAttribute((const void*)attn_kernel, hipFuncAttributeMaxDynamicSharedMemorySize, LDS_BYTES);
  hipLaunchKernelGGL(attn_kernel, dim3(256), dim3(512), LDS_BYTES, stream, p);
  hipLaunchKernelGGL(outproj_naive, dim3(64, 18), dim3(64), 0, stream, p);
}
```

```cpp
#include <hip/hip_runtime.h>
#include <stdint.h>

typedef __bf16 bf16x8 __attribute__((ext_vector_type(8)));
typedef __bf16 bf16x4 __attribute__((ext_vector_type(4)));
typedef __bf16 bf16x2 __attribute__((ext_vector_type(2)));
typedef float f32x16 __attribute__((ext_vector_type(16)));
typedef float f32x4 __attribute__((ext_vector_type(4)));
typedef float f32x2 __attribute__((ext_vector_type(2)));
typedef unsigned u32x4 __attribute__((ext_vector_type(4)));
#define LAS __attribute__((address_space(3)))
#define DI __device__ __forceinline__
#define SB() __builtin_amdgcn_sched_barrier(0)
#define PIN(x) asm volatile("" :: "v"(x))

#define PF 0
constexpr int NB = 8, SQ = 128, DM = 576, NH = 32, SK = 1024, DH = 128;
constexpr int G_OFF = 98304, G_SLOT = 32768;
constexpr int LDS_BYTES = 163840;
constexpr unsigned OOB = 0x70000000u;
constexpr float CSC = 0.12751743082f;
constexpr float L_LO = 5.9604645e-8f;
constexpr float L_HI = 1.0995116e12f;
constexpr float O_HI = 1.2676506e30f;

struct Params {
  const void* x[2]; const void* k[2]; const void* v[2]; const void* wq[2]; const void* wo[2];
  void* o_ws; void* out;
};

DI unsigned cvtpk(float lo, float hi) { f32x2 v = {lo, hi}; bf16x2 b = __builtin_convertvector(v, bf16x2); return __builtin_bit_cast(unsigned, b); }
DI bf16x8 pack8(const f32x16& a, int base) {
  u32x4 w = {cvtpk(a[base + 0], a[base + 1]), cvtpk(a[base + 2], a[base + 3]), cvtpk(a[base + 4], a[base + 5]), cvtpk(a[base + 6], a[base + 7])};
  return __builtin_bit_cast(bf16x8, w);
}
DI bf16x8 pack8s(const f32x16& a, int base, float s) {
  u32x4 w = {cvtpk(a[base + 0] * s, a[base + 1] * s), cvtpk(a[base + 2] * s, a[base + 3] * s), cvtpk(a[base + 4] * s, a[base + 5] * s), cvtpk(a[base + 6] * s, a[base + 7] * s)};
  return __builtin_bit_cast(bf16x8, w);
}
DI float fadd_s(float a, float b) { float r; asm("v_add_f32_e32 %0, %1, %2" : "=v"(r) : "v"(a), "v"(b)); return r; }
DI float max2_s(float a, float b) { return __builtin_elementwise_maximum(a, b); }
DI float swap_max(float v) { auto r = __builtin_amdgcn_permlane32_swap(__float_as_uint(v), __float_as_uint(v), false, false); return max2_s(__uint_as_float(r[0]), __uint_as_float(r[1])); }
DI float swap_sum(float v) { auto r = __builtin_amdgcn_permlane32_swap(__float_as_uint(v), __float_as_uint(v), false, false); return __uint_as_float(r[0]) + __uint_as_float(r[1]); }
DI int opaque_lane() { int l = threadIdx.x; asm volatile("" : "+v"(l)); return l & 63; }
DI u32x4 make_srd(const void* p, unsigned bytes) { const uint64_t a = (uint64_t)p; u32x4 s = {(unsigned)a, (unsigned)(a >> 32) & 0xffffu, bytes, 0x00020000u}; return s; }

template <bool NT> DI void dma1(unsigned m0v, unsigned v, u32x4 srd, unsigned s) {
  if constexpr (NT) asm volatile("s_mov_b32 m0, %0\n\ts_nop 0\n\tbuffer_load_dwordx4 %1, %2, %3 offen nt lds" :: "s"(m0v), "v"(v), "s"(srd), "s"(s) : "memory");
  else asm volatile("s_mov_b32 m0, %0\n\ts_nop 0\n\tbuffer_load_dwordx4 %1, %2, %3 offen lds" :: "s"(m0v), "v"(v), "s"(srd), "s"(s) : "memory");
}
DI void touch1(unsigned m0v, unsigned v, u32x4 srd) {
  asm volatile("s_mov_b32 m0, %0\n\ts_nop 0\n\tbuffer_load_dword %1, %2, 0 offen lds" :: "s"(m0v), "v"(v), "s"(srd) : "memory");
}
template <int N> DI void wait_vm() { asm volatile("s_waitcnt vmcnt(%0)" :: "i"(N) : "memory"); }
DI void bar() { __builtin_amdgcn_s_waitcnt(0xC07F); asm volatile("s_waitcnt lgkmcnt(0)\n\ts_barrier" ::: "memory"); }
DI bf16x8 lds_b128(unsigned addr) { return *(const LAS bf16x8*)(uintptr_t)addr; }
DI bf16x4 lds_tr(unsigned addr) { return __builtin_amdgcn_ds_read_tr16_b64_v4bf16((LAS bf16x4*)(uintptr_t)addr); }
DI bf16x8 cat(bf16x4 a, bf16x4 b) { return __builtin_shufflevector(a, b, 0, 1, 2, 3, 4, 5, 6, 7); }
#define MFMA32(a, b, c) __builtin_amdgcn_mfma_f32_32x32x16_bf16((a), (b), (c), 0, 0, 0)

#define STAMP(i)

struct Ctx {
  unsigned dva, dvb;
  unsigned dga, dgb;
  unsigned kro[2], vtr[2];
  u32x4 srdKV, srdG;
  unsigned kv_s, g_s;
  int w;
};
DI void issue_kv(const Ctx& c, int i, unsigned lds_tile, unsigned so) {
  dma1<true>(lds_tile + (c.w & 3) * 4096 + i * 1024, (i & 2) ? c.dvb : c.dva, c.srdKV, so + (i & 1) * 128u);
}
template <int TEAM> DI void issue_g(const Ctx& c, unsigned so, int i, unsigned gb) {
  if constexpr (TEAM == 0) dma1<false>(gb + (c.w & 3) * 4096 + i * 1024, (i & 1) ? c.dgb : c.dga, c.srdG, so + (unsigned)i * (8 * 1152));
  else dma1<false>(gb + 16384 + (c.w & 3) * 4096 + i * 1024, (i & 2) ? c.dgb : c.dga, c.srdG, so + (i & 1) * 128u);
}
template <int TEAM> DI unsigned g_off(const Ctx& c, int r, int st) {
  if constexpr (TEAM == 0) return c.g_s + (unsigned)st * 128u; else return c.g_s + (unsigned)st * (64 * 8192) + (unsigned)r * (16 * 256);
}
struct QAddr { unsigned x0, x1, w0, w1; };
DI QAddr q_addr(const Ctx& c, unsigned gb) {
  const int lane = opaque_lane();
  const int r32 = lane & 31, hi = lane >> 5, q4 = (lane & 15) >> 2, pp = lane & 3, blk = (lane >> 4) & 1;
  const int psw = ((pp & 1) << 1) | (pp >> 1);
  const unsigned xrow = 32 * (c.w & 3) + r32;
  const unsigned xb = gb + 1024 * (xrow >> 3) + 64 * (xrow & 7);
  QAddr a;
  a.x0 = xb + 16 * (hi ^ ((xrow >> 2) & 3)); a.x1 = xb + 16 * ((2 + hi) ^ ((xrow >> 2) & 3));
  const unsigned wb = gb + 16384 + 2048 * hi + 64 * q4 + 8 * (psw & 1) + 1024 * (c.w >> 2);
  a.w0 = wb + 16 * ((2 * blk + (psw >> 1)) ^ ((2 * hi) & 3)); a.w1 = wb + 256 + 16 * ((2 * blk + (psw >> 1)) ^ ((2 * hi + 1) & 3));
  asm volatile("" : "+v"(a.x0), "+v"(a.x1), "+v"(a.w0), "+v"(a.w1));
  return a;
}
DI bf16x8 q_rdx(const QAddr& a, int ks) { return lds_b128(((ks & 1) ? a.x1 : a.x0) + 512 * (ks >> 1)); }
DI bf16x8 q_rdw(const QAddr& a, int ks, int cc) { return cat(lds_tr(a.w0 + 4096 * ks + 512 * cc), lds_tr(a.w1 + 4096 * ks + 512 * cc)); }
struct QFrag { bf16x8 xf[4]; bf16x8 wf[4][2]; };
template <int KS> DI void q_read_ks(const QAddr& a, QFrag& f) { f.xf[KS] = q_rdx(a, KS); f.wf[KS][0] = q_rdw(a, KS, 0); f.wf[KS][1] = q_rdw(a, KS, 1); }
template <bool RD, class H> DI void q_pipe(const QAddr& an, QFrag& cur, QFrag& nxt, f32x16 (&qa)[2], H&& hook) {
#define QP(KS) \
  qa[0] = MFMA32(cur.wf[KS][0], cur.xf[KS], qa[0]); \
  if constexpr (RD) q_read_ks<KS>(an, nxt); \
  hook(2 * KS); \
  SB(); \
  qa[1] = MFMA32(cur.wf[KS][1], cur.xf[KS], qa[1]); \
  hook(2 * KS + 1); \
  SB();
  QP(0) QP(1) QP(2) QP(3)
#undef QP
}

template <int V> struct IC { static constexpr int value = V; };
struct AState {
  f32x16 o[4];
  f32x16 sa, sb;
  bf16x8 kf[8];
  bf16x4 vl[8], vh[8];
  u32x4 p0, p1;
  float l0, l1;
};
template <int E> DI void sm_add(AState& a, f32x16& s) { if (E & 1) { a.l1 = fadd_s(a.l1, s[E]); PIN(a.l1); } else { a.l0 = fadd_s(a.l0, s[E]); PIN(a.l0); } }
template <int U> DI void sm_slot(AState& a, f32x16& s) {
  if constexpr (U < 16) { s[U] = __builtin_amdgcn_exp2f(s[U]); PIN(s[U]); }
  if constexpr (U >= 4 && U <= 6) { sm_add<2 * (U - 4)>(a, s); sm_add<2 * (U - 4) + 1>(a, s); }
  if constexpr (U >= 7 && U <= 16) sm_add<U - 1>(a, s);
  if constexpr (U >= 2 && U <= 16 && (U & 1) == 0) {
    constexpr int E = U - 1;
    if constexpr (E < 8) { a.p0[E >> 1] = cvtpk(s[E - 1], s[E]); PIN(a.p0[E >> 1]); }
    else { a.p1[(E - 8) >> 1] = cvtpk(s[E - 1], s[E]); PIN(a.p1[(E - 8) >> 1]); }
  }
}
template <class H> DI void phaseA(const Ctx& c, AState& a, f32x16& sn, f32x16& sp, const bf16x8 (&qf)[8], unsigned vb, H&& hook) {
  unsigned v0 = vb + c.vtr[0], v1 = vb + c.vtr[1];
  asm volatile("" : "+v"(v0), "+v"(v1));
#define VRD(f) do { a.vl[f] = lds_tr(v0 + 4096 * ((f) & 1) + 512 * ((f) >> 1)); a.vh[f] = lds_tr(v1 + 4096 * ((f) & 1) + 512 * ((f) >> 1)); } while (0)
#define GA(i) do { \
    if constexpr (i == 0) sn = MFMA32(a.kf[0], qf[0], f32x16{}); else sn = MFMA32(a.kf[i], qf[i], sn); \
    if constexpr (i == 0) { VRD(0); } if constexpr (i == 1) { VRD(2); VRD(4); } if constexpr (i == 2) { VRD(6); } if constexpr (i == 3) { VRD(1); VRD(3); } if constexpr (i == 4) { VRD(5); } if constexpr (i == 5) { VRD(7); } \
    sm_slot<4 + i>(a, sp); \
    hook(IC<i>{}); SB(); } while (0)
  GA(0); GA(1); GA(2); GA(3); GA(4); GA(5); GA(6); GA(7);
#undef GA
#undef VRD
}
template <class H> DI void phaseB(const Ctx& c, AState& a, f32x16& sn, f32x16& sp, unsigned kb, H&& hook) {
  unsigned k0 = kb + c.kro[0], k1 = kb + c.kro[1];
  asm volatile("" : "+v"(k0), "+v"(k1));
#define KRD(f) a.kf[f] = lds_b128((((f) & 1) ? k1 : k0) + 512 * ((f) >> 1))
#define GB(i) do { \
    if constexpr (i == 4) sm_slot<16>(a, sp); \
    { constexpr int f = 2 * (i & 3) + (i >> 2); a.o[i & 3] = MFMA32(cat(a.vl[f], a.vh[f]), __builtin_bit_cast(bf16x8, (i < 4) ? a.p0 : a.p1), a.o[i & 3]); } \
    if constexpr (i == 0) { KRD(0); } if constexpr (i == 1) { KRD(1); } if constexpr (i == 2) { KRD(2); KRD(3); } if constexpr (i == 3) { KRD(4); } if constexpr (i == 4) { KRD(5); KRD(6); } if constexpr (i == 5) { KRD(7); } \
    if constexpr (i < 4) sm_slot<12 + (i & 3)>(a, sp); else sm_slot<(i & 3)>(a, sn); \
    hook(IC<8 + i>{}); SB(); } while (0)
  GB(0); GB(1); GB(2); GB(3); GB(4); GB(5); GB(6); GB(7);
#undef GB
#undef KRD
  PIN(a.l0); PIN(a.l1);
}

template <int TEAM> DI void body(const Params& p, char* lds) {
  const int tid = threadIdx.x, lane = tid & 63;
  const int w = __builtin_amdgcn_readfirstlane(tid >> 6);
  const int qb = w & 3;
  constexpr int team = TEAM;
  const int wg = blockIdx.x, xcd = wg & 7, idx = wg >> 3;
  const int br = xcd >> 2, hq = xcd & 3, b = idx >> 2, hl = idx & 3;
  const unsigned head0 = 4 * hq + hl;
  const unsigned ldsb = (unsigned)(uintptr_t)(LAS char*)lds;
  const unsigned G0 = ldsb + G_OFF, G1 = G0 + G_SLOT;
  const unsigned R0 = ldsb, R1 = R0 + 32768, R2 = R1 + 32768, GX = R2;

  Ctx c;
  c.w = w;
  STAMP(0);
  {
    const unsigned r7 = (lane >> 2) & 7, bit = (lane >> 4) & 1, h4 = 4 * (lane >> 5), l3 = lane & 3;
    if constexpr (team == 0) {
      c.srdG = make_srd(p.x[br], NB * SQ * DM * 2);
      c.dga = r7 * 1152u + (h4 + (l3 ^ bit)) * 16;
      c.dgb = r7 * 1152u + (h4 + (l3 ^ (2 + bit))) * 16;
      c.g_s = (unsigned)b * (SQ * DM * 2) + (unsigned)qb * (32 * 1152);
      c.srdKV = make_srd(p.v[br], NB * NH * SK * DH * 2);
    } else {
      c.srdG = make_srd(p.wq[br], DM * NH * DH * 2);
      c.dga = r7 * 8192u + (h4 + (l3 ^ bit)) * 16;
      c.dgb = (r7 + 8) * 8192u + (h4 + (l3 ^ (2 + bit))) * 16;
      c.g_s = (unsigned)qb * (16 * 8192) + head0 * 256u;
      c.srdKV = make_srd(p.k[br], NB * NH * SK * DH * 2);
    }
    c.dva = r7 * 256u + (h4 + (l3 ^ bit)) * 16;
    c.dvb = (r7 + 8) * 256u + (h4 + (l3 ^ (2 + bit))) * 16;
    c.kv_s = ((unsigned)(b * NH) + head0) * (SK * DH * 2) + (unsigned)qb * (16 * 256);
  }
  const int rot9 = (b + 2 * hl) % 9;
  auto kst = [&](int u) { const int v = u + rot9; return v >= 9 ? v - 9 : v; };
  auto seq_off = [&](int g) -> unsigned { return g < 9 ? g_off<team>(c, 0, kst(g)) : g_off<team>(c, 1, kst(g - 9)); };
#pragma unroll
  for (int i = 0; i < 4; ++i) issue_g<team>(c, seq_off(0), i, GX);
#pragma unroll
  for (int i = 0; i < 4; ++i) issue_g<team>(c, seq_off(1), i, G0);
#pragma unroll
  for (int i = 0; i < 4; ++i) issue_g<team>(c, seq_off(2), i, G1);
  {
    const int r32 = lane & 31, hi = lane >> 5;
    const int q4 = (lane & 15) >> 2, pp = lane & 3, blk = (lane >> 4) & 1;
    const int psw = ((pp & 1) << 1) | (pp >> 1);
#pragma unroll
    for (int j = 0; j < 2; ++j) {
      const unsigned krow = 32 * team + r32;
      c.kro[j] = 16384 + 2048 * (krow >> 3) + 64 * (krow & 7) + 16 * ((2 * j + hi) ^ ((krow >> 2) & 3));
      c.vtr[j] = 2048 * (4 * team + j) + 64 * (4 * hi + q4) + 16 * ((2 * blk + (psw >> 1)) ^ ((2 * j + hi) & 3)) + 8 * (psw & 1);
    }
  }

  auto q_write = [&](f32x16 (&qa)[2], unsigned gb) {
    const int lane = opaque_lane(); asm volatile("" : "+s"(gb));
#pragma unroll
    for (int i = 0; i < 4; ++i) {
      f32x16 t;
#pragma unroll
      for (int e = 0; e < 8; ++e) t[e] = (float)(__bf16)qa[i >> 1][8 * (i & 1) + e];
      *(LAS bf16x8*)(uintptr_t)(gb + (w * 4 + i) * 1024 + lane * 16) = pack8s(t, 0, CSC);
    }
  };
  auto q_load = [&](bf16x8 (&qf)[8], unsigned gb) {
    const int lane = opaque_lane(); asm volatile("" : "+s"(gb));
#pragma unroll
    for (int s = 0; s < 8; ++s) qf[s] = lds_b128(gb + ((qb + 4 * (s >> 2)) * 4 + (s & 3)) * 1024 + lane * 16);
  };
  auto merge_store = [&](int r, f32x16 (&o)[4], float m_ref, float l_sum, unsigned gb) {
    const int lane = opaque_lane(); asm volatile("" : "+s"(gb));
    const int r32 = lane & 31, hi = lane >> 5;
    l_sum = swap_sum(l_sum);
    if (hi == 0) { *(LAS f32x2*)(uintptr_t)(gb + (w * 32 + r32) * 8) = f32x2{m_ref, l_sum}; }
    bar();
    const f32x2 ml = *(const LAS f32x2*)(uintptr_t)(gb + ((w ^ 4) * 32 + r32) * 8);
    const float M = fmaxf(m_ref, ml[0]);
    const float a0 = __builtin_amdgcn_exp2f(m_ref - M), a1 = __builtin_amdgcn_exp2f(ml[0] - M);
    const float lt = l_sum * a0 + ml[1] * a1;
    const float sc = a0 / lt;
    bar();
#pragma unroll
    for (int i = 0; i < 2; ++i)
#pragma unroll
      for (int g = 0; g < 4; ++g) { const f32x16& s = o[(team ? 0 : 2) + i]; *(LAS f32x4*)(uintptr_t)(gb + w * 8192 + (i * 4 + g) * 1024 + lane * 16) = f32x4{s[4 * g] * sc, s[4 * g + 1] * sc, s[4 * g + 2] * sc, s[4 * g + 3] * sc}; }
    bar();
    char* ob = (char*)p.o_ws + ((size_t)((br * NB + b) * NH + 16 * r + head0) * 4 + qb) * 8192;
    float chk = 0.f;
#pragma unroll
    for (int i = 0; i < 2; ++i) {
      const int cc = 2 * team + i;
      f32x16 f;
#pragma unroll
      for (int g = 0; g < 4; ++g) {
        const f32x4 v = *(const LAS f32x4*)(uintptr_t)(gb + (w ^ 4) * 8192 + (i * 4 + g) * 1024 + lane * 16);
#pragma unroll
        for (int e = 0; e < 4; ++e) { f[4 * g + e] = o[cc][4 * g + e] * sc + v[e]; chk += __builtin_fabsf(f[4 * g + e]); }
      }
      *(bf16x8*)(ob + (2 * cc) * 1024 + lane * 16) = pack8(f, 0);
      *(bf16x8*)(ob + (2 * cc + 1) * 1024 + lane * 16) = pack8(f, 8);
    }
    const bool ok = (lt >= L_LO) && (lt <= L_HI) && (chk <= O_HI);
    const unsigned bad = __any(!ok) ? 1u : 0u;
    if (lane == 0) *(LAS unsigned*)(uintptr_t)(gb + (w ^ 4) * 8192) = bad;
  };
  auto any_bad = [&](unsigned gb) -> bool {
    const int lane = opaque_lane();
    const unsigned f = *(const LAS unsigned*)(uintptr_t)(gb + (lane & 7) * 8192);
    return __any(f != 0u);
  };
  auto tile_off = [&](int T) -> unsigned {
    const unsigned o = c.kv_s + (unsigned)T * 16384u + (T >= 16 ? 16u * SK * DH * 2 - 16u * 16384u : 0u); return T <= 31 ? o : OOB; };

  bf16x8 qf[8];
  f32x16 qa[2] = {};
  AState a;
  {
    QFrag fa, fb;
    const QAddr a0 = q_addr(c, 0);
    auto qaddr = [&](unsigned gb) { QAddr a; a.x0 = a0.x0 + gb; a.x1 = a0.x1 + gb; a.w0 = a0.w0 + gb; a.w1 = a0.w1 + gb; asm volatile("" : "+v"(a.x0), "+v"(a.x1), "+v"(a.w0), "+v"(a.w1)); return a; };
    unsigned gcur = GX, gnxt = G0, gnn = G1;
    auto grot = [&]() { const unsigned tmp = gcur; gcur = gnxt; gnxt = gnn; gnn = tmp; };
    wait_vm<8>();
    bar();
    STAMP(8);
    { const QAddr an = qaddr(gcur); q_read_ks<0>(an, fa); q_read_ks<1>(an, fa); q_read_ks<2>(an, fa); q_read_ks<3>(an, fa); }
#define QIT(g, CUR, NXT) do { \
      wait_vm<4>(); \
      bar(); \
      const QAddr an = qaddr(gnxt); const unsigned so = seq_off((g) + 3); \
      q_pipe<true>(an, CUR, NXT, qa, [&](int i) { if (i < 4) issue_g<team>(c, so, i, gcur); }); \
      grot(); } while (0)
#pragma unroll 1
    for (int g = 0; g < 8; g += 2) { QIT(g, fa, fb); QIT(g + 1, fb, fa); }
#undef QIT
    {
      wait_vm<4>();
      bar();
      const QAddr an = qaddr(gnxt);
      q_pipe<true>(an, fa, fb, qa, [&](int i) {
        if constexpr (team == 1) { if (i < 4) issue_kv(c, i, R0, tile_off(0)); else issue_kv(c, i - 4, R0 + 16384, tile_off(1)); }
        else { if (i < 4) issue_kv(c, i, R1, tile_off(0)); } });
    }
    STAMP(1);
    q_write(qa, G1);
    qa[0] = f32x16{}; qa[1] = f32x16{};
    { const QAddr an = qaddr(gnxt); q_pipe<false>(an, fb, fa, qa, [&](int i) { if constexpr (team == 1) { if (i < 4) issue_kv(c, i, R1 + 16384, tile_off(2)); } }); }
  }
  if constexpr (team == 1) wait_vm<8>();
  bar();
  q_load(qf, G1);
  {
    unsigned k0 = R0 - 16384 + c.kro[0], k1 = R0 - 16384 + c.kro[1];
    asm volatile("" : "+v"(k0), "+v"(k1));
#pragma unroll
    for (int i = 0; i < 8; ++i) a.kf[i] = lds_b128(((i & 1) ? k1 : k0) + 512 * (i >> 1));
  }
  STAMP(2);
  unsigned pcur = R0, pnxt = R1, pnn = R2;
  auto rot = [&]() { const unsigned tmp = pcur; pcur = pnxt; pnxt = pnn; pnn = tmp; };
  a.l0 = 0.f; a.l1 = 0.f;
#pragma unroll
  for (int cc = 0; cc < 4; ++cc) a.o[cc] = f32x16{};
  a.sb = f32x16{};
  a.p0 = u32x4{}; a.p1 = u32x4{};

#define KV_HOOK(n) do { if constexpr (n >= 4 && n < 8) issue_kv(c, n - 4, pnn + (team ? 16384 : 0), offKV); } while (0)
#define STEP_HEAD(T) \
      const unsigned offKV = tile_off((T) + (team ? 3 : 1)); \
      wait_vm<4>(); \
      bar();
#define STEP_P(T_, SN, SP) do { const int T = (T_); \
      STEP_HEAD(T) \
      phaseA(c, a, SN, SP, qf, pcur, [&](auto n) { constexpr int N = decltype(n)::value; KV_HOOK(N); }); \
      phaseB(c, a, SN, SP, pcur, [&](auto n) { }); \
      rot(); \
    } while (0)
  const QAddr ga = q_addr(c, G0);
#define GRD(ks, xf, wf0, wf1) do { \
          xf = lds_b128(((ks & 1) ? gx1 : gx0) + 512 * (ks >> 1)); \
          wf0 = cat(lds_tr(gw0 + 4096 * ks), lds_tr(gw1 + 4096 * ks)); \
          wf1 = cat(lds_tr(gw0 + 4096 * ks + 512), lds_tr(gw1 + 4096 * ks + 512)); } while (0)
#define STEP_H(T_, PAR, SN, SP) do { const int T = (T_); \
      STEP_HEAD(T) \
      const int u = (T >> 1) + 1; \
      const unsigned gc = (u & 1) ? 0u : (unsigned)G_SLOT, gi = (u & 1) ? G1 : G0; \
      const unsigned so = u < 8 ? g_off<team>(c, 1, kst(u + 1)) : OOB; \
      unsigned gx0 = ga.x0 + gc, gx1 = ga.x1 + gc, gw0 = ga.w0 + gc, gw1 = ga.w1 + gc; \
      bf16x8 xa, wa0, wa1, xb, wb0, wb1; \
      auto ghook = [&](auto n) { constexpr int N = decltype(n)::value; \
        KV_HOOK(N); \
        if constexpr (PAR == 0 && N < 4) issue_g<team>(c, so, N, gi); \
        if constexpr (N == 14) GRD((2 * PAR), xa, wa0, wa1); \
        if constexpr (N == 15) GRD((2 * PAR + 1), xb, wb0, wb1); \
      }; \
      phaseA(c, a, SN, SP, qf, pcur, ghook); \
      phaseB(c, a, SN, SP, pcur, ghook); \
      qa[0] = MFMA32(wa0, xa, qa[0]); qa[1] = MFMA32(wa1, xa, qa[1]); qa[0] = MFMA32(wb0, xb, qa[0]); qa[1] = MFMA32(wb1, xb, qa[1]); \
      SB(); \
      rot(); \
    } while (0)

#pragma unroll 1
  for (int T2 = 0; T2 < 16; T2 += 2) {
    STEP_H(T2, 0, a.sa, a.sb);
    if (T2 == 2) { STAMP(9); }
    if (T2 == 0) {
      a.l0 = 0.f; a.l1 = 0.f;
#pragma unroll
      for (int cc = 0; cc < 4; ++cc) a.o[cc] = f32x16{};
    }
    STEP_H(T2 + 1, 1, a.sb, a.sa);
  }
  q_write(qa, G0);
  bar();
  q_load(qf, G0);
  STAMP(5);
  auto finish_item = [&](int r) {
    bar();
    merge_store(r, a.o, 0.f, a.l0 + a.l1, G0);
    bar();
    if (any_bad(G0)) {
      float m = -3.0e38f, l = 0.f;
      bf16x8 qs[8];
      if (r == 0) {
        f32x16 q2[2] = {};
#pragma unroll 1
        for (int st = 0; st < 9; ++st) {
          bar();
#pragma unroll
          for (int i = 0; i < 4; ++i) issue_g<team>(c, g_off<team>(c, 0, st), i, G0);
          wait_vm<0>();
          bar();
          const QAddr ga = q_addr(c, G0);
#pragma unroll
          for (int g = 0; g < 8; ++g) q2[g & 1] = MFMA32(q_rdw(ga, g >> 1, g & 1), q_rdx(ga, g >> 1), q2[g & 1]);
        }
        bar();
        q_write(q2, G0);
        bar();
        q_load(qs, G0);
      } else {
#pragma unroll
        for (int i = 0; i < 8; ++i) qs[i] = qf[i];
      }
#pragma unroll
      for (int cc = 0; cc < 4; ++cc) a.o[cc] = f32x16{};
#pragma unroll 1
      for (int t = 0; t < 16; ++t) {
        bar();
        {
          const unsigned o = c.kv_s + (unsigned)r * (16u * SK * DH * 2) + (unsigned)t * 16384u;
#pragma unroll
          for (int i = 0; i < 4; ++i) issue_kv(c, i, G0 + (team ? 16384 : 0), o);
          wait_vm<0>();
        }
        bar();
        unsigned k0 = G0 + c.kro[0], k1 = G0 + c.kro[1], v0 = G0 + c.vtr[0], v1 = G0 + c.vtr[1];
        asm volatile("" : "+v"(k0), "+v"(k1), "+v"(v0), "+v"(v1));
        f32x16 s = {};
#pragma unroll
        for (int i = 0; i < 8; ++i) s = MFMA32(lds_b128(((i & 1) ? k1 : k0) + 512 * (i >> 1)), qs[i], s);
        float mx = s[0];
#pragma unroll
        for (int e = 1; e < 16; ++e) mx = max2_s(mx, s[e]);
        mx = swap_max(mx);
        const float mn = max2_s(m, mx);
        const float f = __builtin_amdgcn_exp2f(m - mn);
        m = mn; l *= f;
#pragma unroll
        for (int e = 0; e < 16; ++e) { s[e] = __builtin_amdgcn_exp2f(s[e] - mn); l += s[e]; }
        const bf16x8 q0 = pack8(s, 0), q1 = pack8(s, 8);
#pragma unroll
        for (int cc = 0; cc < 4; ++cc) {
#pragma unroll
          for (int e = 0; e < 16; ++e) a.o[cc][e] *= f;
#pragma unroll
          for (int h = 0; h < 2; ++h) {
            const int fr = 2 * cc + h;
            a.o[cc] = MFMA32(cat(lds_tr(v0 + 4096 * (fr & 1) + 512 * (fr >> 1)), lds_tr(v1 + 4096 * (fr & 1) + 512 * (fr >> 1))), h ? q1 : q0, a.o[cc]);
          }
        }
      }
      bar();
      merge_store(r, a.o, m, l, G0);
      bar();
    }
  };
#pragma unroll 1
  for (int T2 = 16;; T2 += 2) {
    STEP_P(T2, a.sa, a.sb);
    if (T2 == 16 || T2 == 32) {
      STAMP(T2 == 16 ? 3 : 6);
      finish_item(T2 == 32);
      STAMP(T2 == 16 ? 4 : 7);
      if (T2 == 32) break;
      a.l0 = 0.f; a.l1 = 0.f;
#pragma unroll
      for (int cc = 0; cc < 4; ++cc) a.o[cc] = f32x16{};
    }
    STEP_P(T2 + 1, a.sb, a.sa);
  }
}

__global__ __launch_bounds__(512, 2) void attn_kernel(Params p) {
  extern __shared__ __attribute__((aligned(16))) char lds[];
  if (__builtin_amdgcn_readfirstlane(threadIdx.x >> 8) == 0) body<0>(p, lds); else body<1>(p, lds);
}

__global__ __launch_bounds__(64) void outproj_naive(Params p) {
  const int lane = threadIdx.x, r32 = lane & 31, hi = lane >> 5;
  const int rb = blockIdx.x;
  const int cb = blockIdx.y;
  const int br = rb >> 5, b = (rb >> 2) & 7, qb = rb & 3;
  const unsigned short* wo = (const unsigned short*)p.wo[br];
  f32x16 acc = {};
  for (int h = 0; h < NH; ++h) {
    const char* ob = (const char*)p.o_ws + ((size_t)((br * NB + b) * NH + h) * 4 + qb) * 8192;
    for (int s = 0; s < 8; ++s) {
      const bf16x8 a = *(const bf16x8*)(ob + s * 1024 + lane * 16);
      typedef unsigned short u16x8 __attribute__((ext_vector_type(8)));
      u16x8 bv;
      for (int j = 0; j < 8; ++j) bv[j] = wo[(size_t)(h * 128 + 16 * s + 8 * hi + j) * DM + cb * 32 + r32];
      acc = MFMA32(a, __builtin_bit_cast(bf16x8, bv), acc);
    }
  }
  __bf16* out = (__bf16*)p.out;
  for (int i = 0; i < 16; ++i) {
    const int row = b * SQ + qb * 32 + (i & 3) + 8 * (i >> 2) + 4 * hi;
    out[(size_t)row * 1152 + br * 576 + cb * 32 + r32] = (__bf16)acc[i];
  }
}

extern "C" void kernel_launch(void* const* d_in, const int* in_sizes, int n_in, void* d_out, int out_size, void* d_ws, size_t ws_size, hipStream_t stream) {
  Params p;
  for (int i = 0; i < 2; ++i) { p.x[i] = d_in[5 * i + 0]; p.k[i] = d_in[5 * i + 1]; p.v[i] = d_in[5 * i + 2]; p.wq[i] = d_in[5 * i + 3]; p.wo[i] = d_in[5 * i + 4]; }
  p.o_ws = d_ws; p.out = d_out;
  (void)hipFuncSetAttribute((const void*)attn_kernel, hipFuncAttributeMaxDynamicSharedMemorySize, LDS_BYTES);
  hipLaunchKernelGGL(attn_kernel, dim3(256), dim3(512), LDS_BYTES, stream, p);
  hipLaunchKernelGGL(outproj_naive, dim3(64, 18), dim3(64), 0, stream, p);
}
```

```cpp
#include <hip/hip_runtime.h>
#include <stdint.h>

typedef __bf16 bf16x8 __attribute__((ext_vector_type(8)));
typedef __bf16 bf16x4 __attribute__((ext_vector_type(4)));
typedef __bf16 bf16x2 __attribute__((ext_vector_type(2)));
typedef float f32x16 __attribute__((ext_vector_type(16)));
typedef float f32x4 __attribute__((ext_vector_type(4)));
typedef float f32x2 __attribute__((ext_vector_type(2)));
typedef unsigned u32x4 __attribute__((ext_vector_type(4)));
#define LAS __attribute__((address_space(3)))
#define DI __device__ __forceinline__
#define SB() __builtin_amdgcn_sched_barrier(0)
#define PIN(x) asm volatile("" :: "v"(x))

#define PF 0
constexpr int NB = 8, SQ = 128, DM = 576, NH = 32, SK = 1024, DH = 128;
constexpr int G_OFF = 98304, G_SLOT = 32768;
constexpr int LDS_BYTES = 163840;
constexpr unsigned OOB = 0x70000000u;
constexpr float CSC = 0.12751743082f;
constexpr float L_LO = 5.9604645e-8f;
constexpr float L_HI = 1.0995116e12f;
constexpr float O_HI = 1.2676506e30f;

constexpr unsigned WS_PART = 32u << 20;
constexpr unsigned WS_MB = 60u << 20;
constexpr unsigned WS_BYTES = 64u << 20;
constexpr int WO_SLOT = 36864;
typedef unsigned long long u64;
#define GAS __attribute__((address_space(1)))
struct Params {
  const void* x[2]; const void* k[2]; const void* v[2]; const void* wq[2]; const void* wo[2];
  void* o_ws; void* out;
};

DI unsigned cvtpk(float lo, float hi) { f32x2 v = {lo, hi}; bf16x2 b = __builtin_convertvector(v, bf16x2); return __builtin_bit_cast(unsigned, b); }
DI bf16x8 pack8(const f32x16& a, int base) {
  u32x4 w = {cvtpk(a[base + 0], a[base + 1]), cvtpk(a[base + 2], a[base + 3]), cvtpk(a[base + 4], a[base + 5]), cvtpk(a[base + 6], a[base + 7])};
  return __builtin_bit_cast(bf16x8, w);
}
DI bf16x8 pack8s(const f32x16& a, int base, float s) {
  u32x4 w = {cvtpk(a[base + 0] * s, a[base + 1] * s), cvtpk(a[base + 2] * s, a[base + 3] * s), cvtpk(a[base + 4] * s, a[base + 5] * s), cvtpk(a[base + 6] * s, a[base + 7] * s)};
  return __builtin_bit_cast(bf16x8, w);
}
DI float fadd_s(float a, float b) { float r; asm("v_add_f32_e32 %0, %1, %2" : "=v"(r) : "v"(a), "v"(b)); return r; }
DI float max2_s(float a, float b) { return __builtin_elementwise_maximum(a, b); }
DI float swap_max(float v) { auto r = __builtin_amdgcn_permlane32_swap(__float_as_uint(v), __float_as_uint(v), false, false); return max2_s(__uint_as_float(r[0]), __uint_as_float(r[1])); }
DI float swap_sum(float v) { auto r = __builtin_amdgcn_permlane32_swap(__float_as_uint(v), __float_as_uint(v), false, false); return __uint_as_float(r[0]) + __uint_as_float(r[1]); }
DI int opaque_lane() { int l = threadIdx.x; asm volatile("" : "+v"(l)); return l & 63; }
DI u32x4 make_srd(const void* p, unsigned bytes) { const uint64_t a = (uint64_t)p; u32x4 s = {(unsigned)a, (unsigned)(a >> 32) & 0xffffu, bytes, 0x00020000u}; return s; }

template <bool NT> DI void dma1(unsigned m0v, unsigned v, u32x4 srd, unsigned s) {
  if constexpr (NT) asm volatile("s_mov_b32 m0, %0\n\ts_nop 0\n\tbuffer_load_dwordx4 %1, %2, %3 offen nt lds" :: "s"(m0v), "v"(v), "s"(srd), "s"(s) : "memory");
  else asm volatile("s_mov_b32 m0, %0\n\ts_nop 0\n\tbuffer_load_dwordx4 %1, %2, %3 offen lds" :: "s"(m0v), "v"(v), "s"(srd), "s"(s) : "memory");
}
DI void touch1(unsigned m0v, unsigned v, u32x4 srd) {
  asm volatile("s_mov_b32 m0, %0\n\ts_nop 0\n\tbuffer_load_dword %1, %2, 0 offen lds" :: "s"(m0v), "v"(v), "s"(srd) : "memory");
}
template <int N> DI void wait_vm() { asm volatile("s_waitcnt vmcnt(%0)" :: "i"(N) : "memory"); }
DI void bar() { __builtin_amdgcn_s_waitcnt(0xC07F); asm volatile("s_waitcnt lgkmcnt(0)\n\ts_barrier" ::: "memory"); }
DI bf16x8 lds_b128(unsigned addr) { return *(const LAS bf16x8*)(uintptr_t)addr; }
DI bf16x4 lds_tr(unsigned addr) { return __builtin_amdgcn_ds_read_tr16_b64_v4bf16((LAS bf16x4*)(uintptr_t)addr); }
DI bf16x8 cat(bf16x4 a, bf16x4 b) { return __builtin_shufflevector(a, b, 0, 1, 2, 3, 4, 5, 6, 7); }
#define MFMA32(a, b, c) __builtin_amdgcn_mfma_f32_32x32x16_bf16((a), (b), (c), 0, 0, 0)
#define MFMA16(a, b, c) __builtin_amdgcn_mfma_f32_16x16x32_bf16((a), (b), (c), 0, 0, 0)
DI u64 ld_mb(const char* p) { return __hip_atomic_load((const GAS u64*)p, __ATOMIC_RELAXED, __HIP_MEMORY_SCOPE_AGENT); }
DI void st_mb(char* p, u64 v) { __hip_atomic_store((GAS u64*)p, v, __ATOMIC_RELAXED, __HIP_MEMORY_SCOPE_AGENT); }

#define STAMP(i)

struct Ctx {
  unsigned dva, dvb;
  unsigned dga, dgb;
  unsigned kro[2], vtr[2];
  u32x4 srdKV, srdG;
  unsigned kv_s, g_s;
  int w;
};
DI void issue_kv(const Ctx& c, int i, unsigned lds_tile, unsigned so) {
  dma1<true>(lds_tile + (c.w & 3) * 4096 + i * 1024, (i & 2) ? c.dvb : c.dva, c.srdKV, so + (i & 1) * 128u);
}
template <int TEAM> DI void issue_g(const Ctx& c, unsigned so, int i, unsigned gb) {
  if constexpr (TEAM == 0) dma1<false>(gb + (c.w & 3) * 4096 + i * 1024, (i & 1) ? c.dgb : c.dga, c.srdG, so + (unsigned)i * (8 * 1152));
  else dma1<false>(gb + 16384 + (c.w & 3) * 4096 + i * 1024, (i & 2) ? c.dgb : c.dga, c.srdG, so + (i & 1) * 128u);
}
template <int TEAM> DI unsigned g_off(const Ctx& c, int r, int st) {
  if constexpr (TEAM == 0) return c.g_s + (unsigned)st * 128u; else return c.g_s + (unsigned)st * (64 * 8192) + (unsigned)r * (16 * 256);
}
struct QAddr { unsigned x0, x1, w0, w1; };
DI QAddr q_addr(const Ctx& c, unsigned gb) {
  const int lane = opaque_lane();
  const int r32 = lane & 31, hi = lane >> 5, q4 = (lane & 15) >> 2, pp = lane & 3, blk = (lane >> 4) & 1;
  const int psw = ((pp & 1) << 1) | (pp >> 1);
  const unsigned xrow = 32 * (c.w & 3) + r32;
  const unsigned xb = gb + 1024 * (xrow >> 3) + 64 * (xrow & 7);
  QAddr a;
  a.x0 = xb + 16 * (hi ^ ((xrow >> 2) & 3)); a.x1 = xb + 16 * ((2 + hi) ^ ((xrow >> 2) & 3));
  const unsigned wb = gb + 16384 + 2048 * hi + 64 * q4 + 8 * (psw & 1) + 1024 * (c.w >> 2);
  a.w0 = wb + 16 * ((2 * blk + (psw >> 1)) ^ ((2 * hi) & 3)); a.w1 = wb + 256 + 16 * ((2 * blk + (psw >> 1)) ^ ((2 * hi + 1) & 3));
  asm volatile("" : "+v"(a.x0), "+v"(a.x1), "+v"(a.w0), "+v"(a.w1));
  return a;
}
DI bf16x8 q_rdx(const QAddr& a, int ks) { return lds_b128(((ks & 1) ? a.x1 : a.x0) + 512 * (ks >> 1)); }
DI bf16x8 q_rdw(const QAddr& a, int ks, int cc) { return cat(lds_tr(a.w0 + 4096 * ks + 512 * cc), lds_tr(a.w1 + 4096 * ks + 512 * cc)); }
struct QFrag { bf16x8 xf[4]; bf16x8 wf[4][2]; };
template <int KS> DI void q_read_ks(const QAddr& a, QFrag& f) { f.xf[KS] = q_rdx(a, KS); f.wf[KS][0] = q_rdw(a, KS, 0); f.wf[KS][1] = q_rdw(a, KS, 1); }
template <bool RD, class H> DI void q_pipe(const QAddr& an, QFrag& cur, QFrag& nxt, f32x16 (&qa)[2], H&& hook) {
#define QP(KS) \
  qa[0] = MFMA32(cur.wf[KS][0], cur.xf[KS], qa[0]); \
  if constexpr (RD) q_read_ks<KS>(an, nxt); \
  hook(2 * KS); \
  SB(); \
  qa[1] = MFMA32(cur.wf[KS][1], cur.xf[KS], qa[1]); \
  hook(2 * KS + 1); \
  SB();
  QP(0) QP(1) QP(2) QP(3)
#undef QP
}

template <int V> struct IC { static constexpr int value = V; };
struct AState {
  f32x16 o[4];
  f32x16 sa, sb;
  bf16x8 kf[8];
  bf16x4 vl[8], vh[8];
  u32x4 p0, p1;
  float l0, l1;
};
template <int E> DI void sm_add(AState& a, f32x16& s) { if (E & 1) { a.l1 = fadd_s(a.l1, s[E]); PIN(a.l1); } else { a.l0 = fadd_s(a.l0, s[E]); PIN(a.l0); } }
template <int U> DI void sm_slot(AState& a, f32x16& s) {
  if constexpr (U < 16) { s[U] = __builtin_amdgcn_exp2f(s[U]); PIN(s[U]); }
  if constexpr (U >= 4 && U <= 6) { sm_add<2 * (U - 4)>(a, s); sm_add<2 * (U - 4) + 1>(a, s); }
  if constexpr (U >= 7 && U <= 16) sm_add<U - 1>(a, s);
  if constexpr (U >= 2 && U <= 16 && (U & 1) == 0) {
    constexpr int E = U - 1;
    if constexpr (E < 8) { a.p0[E >> 1] = cvtpk(s[E - 1], s[E]); PIN(a.p0[E >> 1]); }
    else { a.p1[(E - 8) >> 1] = cvtpk(s[E - 1], s[E]); PIN(a.p1[(E - 8) >> 1]); }
  }
}
template <class H> DI void phaseA(const Ctx& c, AState& a, f32x16& sn, f32x16& sp, const bf16x8 (&qf)[8], unsigned vb, H&& hook) {
  unsigned v0 = vb + c.vtr[0], v1 = vb + c.vtr[1];
  asm volatile("" : "+v"(v0), "+v"(v1));
#define VRD(f) do { a.vl[f] = lds_tr(v0 + 4096 * ((f) & 1) + 512 * ((f) >> 1)); a.vh[f] = lds_tr(v1 + 4096 * ((f) & 1) + 512 * ((f) >> 1)); } while (0)
#define GA(i) do { \
    if constexpr (i == 0) sn = MFMA32(a.kf[0], qf[0], f32x16{}); else sn = MFMA32(a.kf[i], qf[i], sn); \
    if constexpr (i == 0) { VRD(0); } if constexpr (i == 1) { VRD(2); VRD(4); } if constexpr (i == 2) { VRD(6); } if constexpr (i == 3) { VRD(1); VRD(3); } if constexpr (i == 4) { VRD(5); } if constexpr (i == 5) { VRD(7); } \
    sm_slot<4 + i>(a, sp); \
    hook(IC<i>{}); SB(); } while (0)
  GA(0); GA(1); GA(2); GA(3); GA(4); GA(5); GA(6); GA(7);
#undef GA
#undef VRD
}
template <class H> DI void phaseB(const Ctx& c, AState& a, f32x16& sn, f32x16& sp, unsigned kb, H&& hook) {
  unsigned k0 = kb + c.kro[0], k1 = kb + c.kro[1];
  asm volatile("" : "+v"(k0), "+v"(k1));
#define KRD(f) a.kf[f] = lds_b128((((f) & 1) ? k1 : k0) + 512 * ((f) >> 1))
#define GB(i) do { \
    if constexpr (i == 4) sm_slot<16>(a, sp); \
    { constexpr int f = 2 * (i & 3) + (i >> 2); a.o[i & 3] = MFMA32(cat(a.vl[f], a.vh[f]), __builtin_bit_cast(bf16x8, (i < 4) ? a.p0 : a.p1), a.o[i & 3]); } \
    if constexpr (i == 0) { KRD(0); } if constexpr (i == 1) { KRD(1); } if constexpr (i == 2) { KRD(2); KRD(3); } if constexpr (i == 3) { KRD(4); } if constexpr (i == 4) { KRD(5); KRD(6); } if constexpr (i == 5) { KRD(7); } \
    if constexpr (i < 4) sm_slot<12 + (i & 3)>(a, sp); else sm_slot<(i & 3)>(a, sn); \
    hook(IC<8 + i>{}); SB(); } while (0)
  GB(0); GB(1); GB(2); GB(3); GB(4); GB(5); GB(6); GB(7);
#undef GB
#undef KRD
  PIN(a.l0); PIN(a.l1);
}

template <int TEAM> DI void body(const Params& p, char* lds) {
  const int tid = threadIdx.x, lane = tid & 63;
  const int w = __builtin_amdgcn_readfirstlane(tid >> 6);
  const int qb = w & 3;
  constexpr int team = TEAM;
  const int wg = blockIdx.x, xcd = wg & 7, idx = wg >> 3;
  const int br = xcd >> 2, hq = xcd & 3, b = idx >> 2, hl = idx & 3;
  const unsigned head0 = 4 * hq + hl;
  const unsigned ldsb = (unsigned)(uintptr_t)(LAS char*)lds;
  const unsigned G0 = ldsb + G_OFF, G1 = G0 + G_SLOT;
  const unsigned R0 = ldsb, R1 = R0 + 32768, R2 = R1 + 32768, GX = R2;

  Ctx c;
  c.w = w;
  STAMP(0);
  char* const ws = (char*)p.o_ws;
  u64 nonce = __builtin_amdgcn_s_memrealtime();
  if (tid == 0) { st_mb(ws + WS_MB + wg * 128, nonce); *(LAS u64*)(uintptr_t)R1 = nonce; }
  const __amdgpu_buffer_rsrc_t rsW = __builtin_amdgcn_make_buffer_rsrc(ws, 0, WS_BYTES, 0x00020000);
  {
    const unsigned r7 = (lane >> 2) & 7, bit = (lane >> 4) & 1, h4 = 4 * (lane >> 5), l3 = lane & 3;
    if constexpr (team == 0) {
      c.srdG = make_srd(p.x[br], NB * SQ * DM * 2);
      c.dga = r7 * 1152u + (h4 + (l3 ^ bit)) * 16;
      c.dgb = r7 * 1152u + (h4 + (l3 ^ (2 + bit))) * 16;
      c.g_s = (unsigned)b * (SQ * DM * 2) + (unsigned)qb * (32 * 1152);
      c.srdKV = make_srd(p.v[br], NB * NH * SK * DH * 2);
    } else {
      c.srdG = make_srd(p.wq[br], DM * NH * DH * 2);
      c.dga = r7 * 8192u + (h4 + (l3 ^ bit)) * 16;
      c.dgb = (r7 + 8) * 8192u + (h4 + (l3 ^ (2 + bit))) * 16;
      c.g_s = (unsigned)qb * (16 * 8192) + head0 * 256u;
      c.srdKV = make_srd(p.k[br], NB * NH * SK * DH * 2);
    }
    c.dva = r7 * 256u + (h4 + (l3 ^ bit)) * 16;
    c.dvb = (r7 + 8) * 256u + (h4 + (l3 ^ (2 + bit))) * 16;
    c.kv_s = ((unsigned)(b * NH) + head0) * (SK * DH * 2) + (unsigned)qb * (16 * 256);
  }
  const int rot9 = (b + 2 * hl) % 9;
  auto kst = [&](int u) { const int v = u + rot9; return v >= 9 ? v - 9 : v; };
  auto seq_off = [&](int g) -> unsigned { return g < 9 ? g_off<team>(c, 0, kst(g)) : g_off<team>(c, 1, kst(g - 9)); };
#pragma unroll
  for (int i = 0; i < 4; ++i) issue_g<team>(c, seq_off(0), i, GX);
#pragma unroll
  for (int i = 0; i < 4; ++i) issue_g<team>(c, seq_off(1), i, G0);
#pragma unroll
  for (int i = 0; i < 4; ++i) issue_g<team>(c, seq_off(2), i, G1);
  {
    const int r32 = lane & 31, hi = lane >> 5;
    const int q4 = (lane & 15) >> 2, pp = lane & 3, blk = (lane >> 4) & 1;
    const int psw = ((pp & 1) << 1) | (pp >> 1);
#pragma unroll
    for (int j = 0; j < 2; ++j) {
      const unsigned krow = 32 * team + r32;
      c.kro[j] = 16384 + 2048 * (krow >> 3) + 64 * (krow & 7) + 16 * ((2 * j + hi) ^ ((krow >> 2) & 3));
      c.vtr[j] = 2048 * (4 * team + j) + 64 * (4 * hi + q4) + 16 * ((2 * blk + (psw >> 1)) ^ ((2 * j + hi) & 3)) + 8 * (psw & 1);
    }
  }

  auto q_write = [&](f32x16 (&qa)[2], unsigned gb) {
    const int lane = opaque_lane(); asm volatile("" : "+s"(gb));
#pragma unroll
    for (int i = 0; i < 4; ++i) {
      f32x16 t;
#pragma unroll
      for (int e = 0; e < 8; ++e) t[e] = (float)(__bf16)qa[i >> 1][8 * (i & 1) + e];
      *(LAS bf16x8*)(uintptr_t)(gb + (w * 4 + i) * 1024 + lane * 16) = pack8s(t, 0, CSC);
    }
  };
  auto q_load = [&](bf16x8 (&qf)[8], unsigned gb) {
    const int lane = opaque_lane(); asm volatile("" : "+s"(gb));
#pragma unroll
    for (int s = 0; s < 8; ++s) qf[s] = lds_b128(gb + ((qb + 4 * (s >> 2)) * 4 + (s & 3)) * 1024 + lane * 16);
  };
  bf16x8 fo[4];
  auto merge_store = [&](int r, f32x16 (&o)[4], float m_ref, float l_sum, unsigned gb) {
    const int lane = opaque_lane(); asm volatile("" : "+s"(gb));
    const int r32 = lane & 31, hi = lane >> 5;
    l_sum = swap_sum(l_sum);
    if (hi == 0) { *(LAS f32x2*)(uintptr_t)(gb + (w * 32 + r32) * 8) = f32x2{m_ref, l_sum}; }
    bar();
    const f32x2 ml = *(const LAS f32x2*)(uintptr_t)(gb + ((w ^ 4) * 32 + r32) * 8);
    const float M = fmaxf(m_ref, ml[0]);
    const float a0 = __builtin_amdgcn_exp2f(m_ref - M), a1 = __builtin_amdgcn_exp2f(ml[0] - M);
    const float lt = l_sum * a0 + ml[1] * a1;
    const float sc = a0 / lt;
    bar();
#pragma unroll
    for (int i = 0; i < 2; ++i)
#pragma unroll
      for (int g = 0; g < 4; ++g) { const f32x16& s = o[(team ? 0 : 2) + i]; *(LAS f32x4*)(uintptr_t)(gb + w * 8192 + (i * 4 + g) * 1024 + lane * 16) = f32x4{s[4 * g] * sc, s[4 * g + 1] * sc, s[4 * g + 2] * sc, s[4 * g + 3] * sc}; }
    bar();
    const unsigned ob = (unsigned)(((br * NB + b) * NH + 16 * r + head0) * 4 + qb) * 8192u;
    float chk = 0.f;
#pragma unroll
    for (int i = 0; i < 2; ++i) {
      const int cc = 2 * team + i;
      f32x16 f;
#pragma unroll
      for (int g = 0; g < 4; ++g) {
        const f32x4 v = *(const LAS f32x4*)(uintptr_t)(gb + (w ^ 4) * 8192 + (i * 4 + g) * 1024 + lane * 16);
#pragma unroll
        for (int e = 0; e < 4; ++e) { f[4 * g + e] = o[cc][4 * g + e] * sc + v[e]; chk += __builtin_fabsf(f[4 * g + e]); }
      }
      fo[2 * i] = pack8(f, 0); fo[2 * i + 1] = pack8(f, 8);
      __builtin_amdgcn_raw_buffer_store_b128(__builtin_bit_cast(u32x4, fo[2 * i]), rsW, lane * 16 + (2 * cc) * 1024, ob, 16);
      __builtin_amdgcn_raw_buffer_store_b128(__builtin_bit_cast(u32x4, fo[2 * i + 1]), rsW, lane * 16 + (2 * cc + 1) * 1024, ob, 16);
    }
    const bool ok = (lt >= L_LO) && (lt <= L_HI) && (chk <= O_HI);
    const unsigned bad = __any(!ok) ? 1u : 0u;
    if (lane == 0) *(LAS unsigned*)(uintptr_t)(gb + (w ^ 4) * 8192) = bad;
  };
  auto any_bad = [&](unsigned gb) -> bool {
    const int lane = opaque_lane();
    const unsigned f = *(const LAS unsigned*)(uintptr_t)(gb + (lane & 7) * 8192);
    return __any(f != 0u);
  };
  auto tile_off = [&](int T) -> unsigned {
    const unsigned o = c.kv_s + (unsigned)T * 16384u + (T >= 16 ? 16u * SK * DH * 2 - 16u * 16384u : 0u); return T <= 31 ? o : OOB; };

  bf16x8 qf[8];
  f32x16 qa[2] = {};
  AState a;
  {
    QFrag fa, fb;
    const QAddr a0 = q_addr(c, 0);
    auto qaddr = [&](unsigned gb) { QAddr a; a.x0 = a0.x0 + gb; a.x1 = a0.x1 + gb; a.w0 = a0.w0 + gb; a.w1 = a0.w1 + gb; asm volatile("" : "+v"(a.x0), "+v"(a.x1), "+v"(a.w0), "+v"(a.w1)); return a; };
    unsigned gcur = GX, gnxt = G0, gnn = G1;
    auto grot = [&]() { const unsigned tmp = gcur; gcur = gnxt; gnxt = gnn; gnn = tmp; };
    wait_vm<8>();
    bar();
    { const u64 n = *(const LAS u64*)(uintptr_t)R1;
      nonce = ((u64)(unsigned)__builtin_amdgcn_readfirstlane((unsigned)(n >> 32)) << 32) | (u64)(unsigned)__builtin_amdgcn_readfirstlane((unsigned)n); }
    STAMP(8);
    { const QAddr an = qaddr(gcur); q_read_ks<0>(an, fa); q_read_ks<1>(an, fa); q_read_ks<2>(an, fa); q_read_ks<3>(an, fa); }
#define QIT(g, CUR, NXT) do { \
      wait_vm<4>(); \
      bar(); \
      const QAddr an = qaddr(gnxt); const unsigned so = seq_off((g) + 3); \
      q_pipe<true>(an, CUR, NXT, qa, [&](int i) { if (i < 4) issue_g<team>(c, so, i, gcur); }); \
      grot(); } while (0)
#pragma unroll 1
    for (int g = 0; g < 8; g += 2) { QIT(g, fa, fb); QIT(g + 1, fb, fa); }
#undef QIT
    {
      wait_vm<4>();
      bar();
      const QAddr an = qaddr(gnxt);
      q_pipe<true>(an, fa, fb, qa, [&](int i) {
        if constexpr (team == 1) { if (i < 4) issue_kv(c, i, R0, tile_off(0)); else issue_kv(c, i - 4, R0 + 16384, tile_off(1)); }
        else { if (i < 4) issue_kv(c, i, R1, tile_off(0)); } });
    }
    STAMP(1);
    q_write(qa, G1);
    qa[0] = f32x16{}; qa[1] = f32x16{};
    { const QAddr an = qaddr(gnxt); q_pipe<false>(an, fb, fa, qa, [&](int i) { if constexpr (team == 1) { if (i < 4) issue_kv(c, i, R1 + 16384, tile_off(2)); } }); }
  }
  if constexpr (team == 1) wait_vm<8>();
  bar();
  q_load(qf, G1);
  {
    unsigned k0 = R0 - 16384 + c.kro[0], k1 = R0 - 16384 + c.kro[1];
    asm volatile("" : "+v"(k0), "+v"(k1));
#pragma unroll
    for (int i = 0; i < 8; ++i) a.kf[i] = lds_b128(((i & 1) ? k1 : k0) + 512 * (i >> 1));
  }
  STAMP(2);
  unsigned pcur = R0, pnxt = R1, pnn = R2;
  auto rot = [&]() { const unsigned tmp = pcur; pcur = pnxt; pnxt = pnn; pnn = tmp; };
  a.l0 = 0.f; a.l1 = 0.f;
#pragma unroll
  for (int cc = 0; cc < 4; ++cc) a.o[cc] = f32x16{};
  a.sb = f32x16{};
  a.p0 = u32x4{}; a.p1 = u32x4{};

#define KV_HOOK(n) do { if constexpr (n >= 4 && n < 8) issue_kv(c, n - 4, pnn + (team ? 16384 : 0), offKV); } while (0)
#define STEP_HEAD(T) \
      const unsigned offKV = tile_off((T) + (team ? 3 : 1)); \
      wait_vm<4>(); \
      bar();
#define STEP_P(T_, SN, SP) do { const int T = (T_); \
      STEP_HEAD(T) \
      phaseA(c, a, SN, SP, qf, pcur, [&](auto n) { constexpr int N = decltype(n)::value; KV_HOOK(N); }); \
      phaseB(c, a, SN, SP, pcur, [&](auto n) { }); \
      rot(); \
    } while (0)
  const QAddr ga = q_addr(c, G0);
#define GRD(ks, xf, wf0, wf1) do { \
          xf = lds_b128(((ks & 1) ? gx1 : gx0) + 512 * (ks >> 1)); \
          wf0 = cat(lds_tr(gw0 + 4096 * ks), lds_tr(gw1 + 4096 * ks)); \
          wf1 = cat(lds_tr(gw0 + 4096 * ks + 512), lds_tr(gw1 + 4096 * ks + 512)); } while (0)
#define STEP_H(T_, PAR, SN, SP) do { const int T = (T_); \
      STEP_HEAD(T) \
      const int u = (T >> 1) + 1; \
      const unsigned gc = (u & 1) ? 0u : (unsigned)G_SLOT, gi = (u & 1) ? G1 : G0; \
      const unsigned so = u < 8 ? g_off<team>(c, 1, kst(u + 1)) : OOB; \
      unsigned gx0 = ga.x0 + gc, gx1 = ga.x1 + gc, gw0 = ga.w0 + gc, gw1 = ga.w1 + gc; \
      bf16x8 xa, wa0, wa1, xb, wb0, wb1; \
      auto ghook = [&](auto n) { constexpr int N = decltype(n)::value; \
        KV_HOOK(N); \
        if constexpr (PAR == 0 && N < 4) issue_g<team>(c, so, N, gi); \
        if constexpr (N == 14) GRD((2 * PAR), xa, wa0, wa1); \
        if constexpr (N == 15) GRD((2 * PAR + 1), xb, wb0, wb1); \
      }; \
      phaseA(c, a, SN, SP, qf, pcur, ghook); \
      phaseB(c, a, SN, SP, pcur, ghook); \
      qa[0] = MFMA32(wa0, xa, qa[0]); qa[1] = MFMA32(wa1, xa, qa[1]); qa[0] = MFMA32(wb0, xb, qa[0]); qa[1] = MFMA32(wb1, xb, qa[1]); \
      SB(); \
      rot(); \
    } while (0)

#pragma unroll 1
  for (int T2 = 0; T2 < 16; T2 += 2) {
    STEP_H(T2, 0, a.sa, a.sb);
    if (T2 == 2) { STAMP(9); }
    if (T2 == 0) {
      a.l0 = 0.f; a.l1 = 0.f;
#pragma unroll
      for (int cc = 0; cc < 4; ++cc) a.o[cc] = f32x16{};
    }
    STEP_H(T2 + 1, 1, a.sb, a.sa);
  }
  q_write(qa, G0);
  bar();
  q_load(qf, G0);
  STAMP(5);
  auto wg_of = [&](int kg_, int hl_) { return (b * 4 + hl_) * 8 + br * 4 + kg_; };
  auto publish = [&](int ph) {
    if (w == 0) {
      const int l = opaque_lane();
      if (l < 4) {
        const int peer = ph < 2 ? wg_of(hq, l) : wg_of(l, hl);
        const u64 n = ld_mb(ws + WS_MB + peer * 128);
        st_mb(ws + WS_MB + 32768 + peer * 128 + ph * 32 + (ph < 2 ? hl : hq) * 8, n);
      }
    }
  };
  auto await = [&](int ph) {
    const int l = opaque_lane();
    const char* a = ws + WS_MB + 32768 + wg * 128 + ph * 32 + (l & 3) * 8;
#pragma unroll 1
    for (int spin = 0; spin < (1 << 22); ++spin) {
      const u64 v = ld_mb(a);
      if (__all(v == nonce)) break;
      __builtin_amdgcn_s_sleep(1);
    }
  };
  auto finish_item = [&](int r) {
    merge_store(r, a.o, 0.f, a.l0 + a.l1, G0);
    bar();
    if (any_bad(G0)) {
      float m = -3.0e38f, l = 0.f;
      bf16x8 qs[8];
      if (r == 0) {
        f32x16 q2[2] = {};
#pragma unroll 1
        for (int st = 0; st < 9; ++st) {
          bar();
#pragma unroll
          for (int i = 0; i < 4; ++i) issue_g<team>(c, g_off<team>(c, 0, st), i, G0);
          wait_vm<0>();
          bar();
          const QAddr ga = q_addr(c, G0);
#pragma unroll
          for (int g = 0; g < 8; ++g) q2[g & 1] = MFMA32(q_rdw(ga, g >> 1, g & 1), q_rdx(ga, g >> 1), q2[g & 1]);
        }
        bar();
        q_write(q2, G0);
        bar();
        q_load(qs, G0);
      } else {
#pragma unroll
        for (int i = 0; i < 8; ++i) qs[i] = qf[i];
      }
#pragma unroll
      for (int cc = 0; cc < 4; ++cc) a.o[cc] = f32x16{};
#pragma unroll 1
      for (int t = 0; t < 16; ++t) {
        bar();
        {
          const unsigned o = c.kv_s + (unsigned)r * (16u * SK * DH * 2) + (unsigned)t * 16384u;
#pragma unroll
          for (int i = 0; i < 4; ++i) issue_kv(c, i, G0 + (team ? 16384 : 0), o);
          wait_vm<0>();
        }
        bar();
        unsigned k0 = G0 + c.kro[0], k1 = G0 + c.kro[1], v0 = G0 + c.vtr[0], v1 = G0 + c.vtr[1];
        asm volatile("" : "+v"(k0), "+v"(k1), "+v"(v0), "+v"(v1));
        f32x16 s = {};
#pragma unroll
        for (int i = 0; i < 8; ++i) s = MFMA32(lds_b128(((i & 1) ? k1 : k0) + 512 * (i >> 1)), qs[i], s);
        float mx = s[0];
#pragma unroll
        for (int e = 1; e < 16; ++e) mx = max2_s(mx, s[e]);
        mx = swap_max(mx);
        const float mn = max2_s(m, mx);
        const float f = __builtin_amdgcn_exp2f(m - mn);
        m = mn; l *= f;
#pragma unroll
        for (int e = 0; e < 16; ++e) { s[e] = __builtin_amdgcn_exp2f(s[e] - mn); l += s[e]; }
        const bf16x8 q0 = pack8(s, 0), q1 = pack8(s, 8);
#pragma unroll
        for (int cc = 0; cc < 4; ++cc) {
#pragma unroll
          for (int e = 0; e < 16; ++e) a.o[cc][e] *= f;
#pragma unroll
          for (int h = 0; h < 2; ++h) {
            const int fr = 2 * cc + h;
            a.o[cc] = MFMA32(cat(lds_tr(v0 + 4096 * (fr & 1) + 512 * (fr >> 1)), lds_tr(v1 + 4096 * (fr & 1) + 512 * (fr >> 1))), h ? q1 : q0, a.o[cc]);
          }
        }
      }
      bar();
      merge_store(r, a.o, m, l, G0);
      bar();
    }
    wait_vm<0>();
    bar();
    publish(r);
  };
  auto seq_head = [&](int q) {
    return q == 0 ? 16 + 4 * hq + hl : (q <= 4 ? 4 * hq + ((hl + q - 1) & 3) : 16 + 4 * hq + ((hl + q - 4) & 3));
  };
  auto wo_issue = [&](int q) {
    const unsigned sl = ldsb + (unsigned)(q & 3) * WO_SLOT;
    const unsigned so = (unsigned)seq_head(q) * (128u * 1152u);
    const int lane = opaque_lane();
    const unsigned r7 = (lane >> 2) & 7, bit = (lane >> 4) & 1, h4 = 4 * (lane >> 5), l3 = lane & 3;
    const u32x4 srdO = make_srd(p.wo[br], NH * DH * DM * 2);
    const unsigned woa = r7 * 1152u + (h4 + (l3 ^ bit)) * 16;
    const unsigned wob = (r7 + 8) * 1152u + (h4 + (l3 ^ (2 + bit))) * 16;
    const unsigned woe = (lane >> 1) * 1152u + (lane & 1) * 16;
#pragma unroll
    for (int i = 0; i < 4; ++i)
      dma1<false>(sl + team * 16384 + qb * 4096 + i * 1024, (i & 2) ? wob : woa, srdO, so + (unsigned)(64 * team + 16 * qb) * 1152u + hl * 256u + (i & 1) * 128u);
    if constexpr (team == 0) dma1<false>(sl + 32768 + qb * 1024, woe, srdO, so + (unsigned)(32 * qb) * 1152u + 1024u + hl * 32u);
  };
#pragma unroll 1
  for (int T2 = 16;; T2 += 2) {
    STEP_P(T2, a.sa, a.sb);
    if (T2 == 16 || T2 == 32) {
      STAMP(T2 == 16 ? 3 : 6);
      bar();
      if (T2 == 32) { wo_issue(0); wo_issue(1); }
      finish_item(T2 == 32);
      STAMP(T2 == 16 ? 4 : 7);
      if (T2 == 32) break;
      a.l0 = 0.f; a.l1 = 0.f;
#pragma unroll
      for (int cc = 0; cc < 4; ++cc) a.o[cc] = f32x16{};
    }
    STEP_P(T2 + 1, a.sb, a.sa);
  }
  {
    constexpr int NX = team ? 4 : 5;
    wo_issue(2); wo_issue(3);
    f32x16 om[4] = {};
    f32x4 oe[2] = {};
    bf16x8 F[4], Fn[4];
#pragma unroll
    for (int i = 0; i < 4; ++i) F[i] = fo[i];
    const int lane = opaque_lane();
    const QAddr g0 = q_addr(c, 0);
    const unsigned wl0 = ldsb + g0.w0 - 16384 - 1024 * team + 16384 * team, wl1 = ldsb + g0.w1 - 16384 - 1024 * team + 16384 * team;
    const unsigned c4 = lane >> 4, i16 = lane & 15;
    const unsigned we = ldsb + 32768 + (64 * team + 16 * (c4 & 1) + 8 * (c4 >> 1) + (i16 >> 2)) * 32 + (i16 & 3) * 8;
    const unsigned fvo = lane * 16 + qb * 8192 + team * 4096;
    auto f_load = [&](int q) {
      const unsigned so = (unsigned)((br * NB + b) * NH + seq_head(q)) * 32768u;
#pragma unroll
      for (int i = 0; i < 4; ++i) Fn[i] = __builtin_bit_cast(bf16x8, __builtin_amdgcn_raw_buffer_load_b128(rsW, fvo + i * 1024, so, 16));
    };
    await(0);
    publish(0);
#pragma unroll 1
    for (int q = 0; q < 8; ++q) {
      if (q == 4) { await(1); publish(1); }
      if (q <= 5) wait_vm<2 * NX>(); else if (q == 6) wait_vm<NX>(); else wait_vm<0>();
      bar();
      if (q >= 1 && q + 3 < 8) wo_issue(q + 3);
      if (q < 7) f_load(q + 1);
      unsigned sl = (unsigned)(q & 3) * WO_SLOT;
      unsigned a0 = wl0 + sl, a1 = wl1 + sl, ae = we + sl;
      asm volatile("" : "+v"(a0), "+v"(a1), "+v"(ae));
#pragma unroll
      for (int ks = 0; ks < 4; ++ks)
#pragma unroll
        for (int nb = 0; nb < 4; ++nb) om[nb] = MFMA32(cat(lds_tr(a0 + 4096 * ks + 512 * nb), lds_tr(a1 + 4096 * ks + 512 * nb)), F[ks], om[nb]);
#pragma unroll
      for (int kk = 0; kk < 2; ++kk) {
        u32x4 f0 = __builtin_bit_cast(u32x4, F[2 * kk]), f1 = __builtin_bit_cast(u32x4, F[2 * kk + 1]);
#pragma unroll
        for (int e = 0; e < 4; ++e) { auto r = __builtin_amdgcn_permlane16_swap(f0[e], f1[e], false, false); f0[e] = r[0]; f1[e] = r[1]; }
        const bf16x8 ax = cat(lds_tr(ae + 1024 * kk), lds_tr(ae + 1024 * kk + 128));
        oe[0] = MFMA16(ax, __builtin_bit_cast(bf16x8, f0), oe[0]);
        oe[1] = MFMA16(ax, __builtin_bit_cast(bf16x8, f1), oe[1]);
      }
#pragma unroll
      for (int i = 0; i < 4; ++i) F[i] = Fn[i];
    }
    STAMP(10);
    bar();
    {
      const unsigned xb = ldsb + w * 9216 + lane * 16;
#pragma unroll
      for (int i = 0; i < 2; ++i)
#pragma unroll
        for (int g = 0; g < 4; ++g) { const f32x16& t = om[(team ? 0 : 2) + i]; *(LAS f32x4*)(uintptr_t)(xb + (i * 4 + g) * 1024) = f32x4{t[4 * g], t[4 * g + 1], t[4 * g + 2], t[4 * g + 3]}; }
      *(LAS f32x4*)(uintptr_t)(xb + 8192) = oe[team ? 0 : 1];
    }
    bar();
    f32x16 r2[2]; f32x4 re;
    {
      const unsigned xb = ldsb + (w ^ 4) * 9216 + lane * 16;
#pragma unroll
      for (int i = 0; i < 2; ++i)
#pragma unroll
        for (int g = 0; g < 4; ++g) { const f32x4 v = *(const LAS f32x4*)(uintptr_t)(xb + (i * 4 + g) * 1024);
#pragma unroll
          for (int e = 0; e < 4; ++e) r2[i][4 * g + e] = om[2 * team + i][4 * g + e] + v[e]; }
      re = oe[team] + *(const LAS f32x4*)(uintptr_t)(xb + 8192);
    }
    const unsigned pbase = WS_PART + (unsigned)(((br * NB + b) * 4 + hl) * 16) * (8u * 2304u) + (unsigned)w * 2304u;
#pragma unroll
    for (int k = 0; k < 4; ++k) {
      if (k != hq) {
        const unsigned so = pbase + (unsigned)(k * 4 + hq) * (8u * 2304u);
        const f32x16& t = r2[k >> 1];
        const int o8 = 8 * (k & 1);
        __builtin_amdgcn_raw_buffer_store_b128(__builtin_bit_cast(u32x4, f32x4{t[o8], t[o8 + 1], t[o8 + 2], t[o8 + 3]}), rsW, lane * 16, so, 16);
        __builtin_amdgcn_raw_buffer_store_b128(__builtin_bit_cast(u32x4, f32x4{t[o8 + 4], t[o8 + 5], t[o8 + 6], t[o8 + 7]}), rsW, lane * 16 + 1024, so, 16);
        __builtin_amdgcn_raw_buffer_store_b32(__float_as_uint(re[k]), rsW, lane * 4 + 2048, so, 16);
      }
    }
    wait_vm<0>();
    bar();
    publish(2);
    float u[8], ue;
    {
      f32x16 t = hq & 2 ? r2[1] : r2[0];
#pragma unroll
      for (int e = 0; e < 8; ++e) u[e] = (hq & 1) ? t[8 + e] : t[e];
      ue = hq == 0 ? re[0] : (hq == 1 ? re[1] : (hq == 2 ? re[2] : re[3]));
    }
    await(2);
    publish(2);
    STAMP(11);
    float s8[8] = {}, se = 0.f;
#pragma unroll
    for (int k = 0; k < 4; ++k) {
      f32x4 v0, v1; float ve;
      if (k == hq) { v0 = f32x4{u[0], u[1], u[2], u[3]}; v1 = f32x4{u[4], u[5], u[6], u[7]}; ve = ue; }
      else {
        const unsigned so = pbase + (unsigned)(hq * 4 + k) * (8u * 2304u);
        v0 = __builtin_bit_cast(f32x4, __builtin_amdgcn_raw_buffer_load_b128(rsW, lane * 16, so, 16));
        v1 = __builtin_bit_cast(f32x4, __builtin_amdgcn_raw_buffer_load_b128(rsW, lane * 16 + 1024, so, 16));
        ve = __uint_as_float(__builtin_amdgcn_raw_buffer_load_b32(rsW, lane * 4 + 2048, so, 16));
      }
#pragma unroll
      for (int e = 0; e < 4; ++e) { s8[e] += v0[e]; s8[4 + e] += v1[e]; }
      se += ve;
    }
    __bf16* out = (__bf16*)p.out;
    {
      const int row = b * SQ + 32 * qb + (lane & 31);
      const int n0 = br * DM + 128 * hl + 32 * (2 * team + (hq >> 1)) + 16 * (hq & 1) + 8 * (lane >> 5);
      u32x4 pk = {cvtpk(s8[0], s8[1]), cvtpk(s8[2], s8[3]), cvtpk(s8[4], s8[5]), cvtpk(s8[6], s8[7])};
      *(u32x4*)(out + (size_t)row * (2 * DM) + n0) = pk;
      const int rowe = b * SQ + 32 * qb + 16 * team + (lane & 15);
      out[(size_t)rowe * (2 * DM) + br * DM + 512 + 16 * hl + 4 * (lane >> 4) + hq] = (__bf16)se;
    }
    STAMP(12);
  }
}

__global__ __launch_bounds__(512, 2) void attn_kernel(Params p) {
  extern __shared__ __attribute__((aligned(16))) char lds[];
  if (__builtin_amdgcn_readfirstlane(threadIdx.x >> 8) == 0) body<0>(p, lds); else body<1>(p, lds);
}

extern "C" void kernel_launch(void* const* d_in, const int* in_sizes, int n_in, void* d_out, int out_size, void* d_ws, size_t ws_size, hipStream_t stream) {
  Params p;
  for (int i = 0; i < 2; ++i) { p.x[i] = d_in[5 * i + 0]; p.k[i] = d_in[5 * i + 1]; p.v[i] = d_in[5 * i + 2]; p.wq[i] = d_in[5 * i + 3]; p.wo[i] = d_in[5 * i + 4]; }
  p.o_ws = d_ws; p.out = d_out;
  (void)hipFuncSetAttribute((const void*)attn_kernel, hipFuncAttributeMaxDynamicSharedMemorySize, LDS_BYTES);
  hipLaunchKernelGGL(attn_kernel, dim3(256), dim3(512), LDS_BYTES, stream, p);
}
```

```cpp
#include <hip/hip_runtime.h>
#include <stdint.h>

typedef __bf16 bf16x8 __attribute__((ext_vector_type(8)));
typedef __bf16 bf16x4 __attribute__((ext_vector_type(4)));
typedef __bf16 bf16x2 __attribute__((ext_vector_type(2)));
typedef float f32x16 __attribute__((ext_vector_type(16)));
typedef float f32x4 __attribute__((ext_vector_type(4)));
typedef float f32x2 __attribute__((ext_vector_type(2)));
typedef unsigned u32x4 __attribute__((ext_vector_type(4)));
#define LAS __attribute__((address_space(3)))
#define DI __device__ __forceinline__
#define SB() __builtin_amdgcn_sched_barrier(0)
#define PIN(x) asm volatile("" :: "v"(x))

#define PF 0
constexpr bool G_NT = false;
constexpr bool EARLY = true;
#define OAUX 16
#define XNO 0
constexpr int NB = 8, SQ = 128, DM = 576, NH = 32, SK = 1024, DH = 128;
constexpr int G_OFF = 98304, G_SLOT = 32768;
constexpr int LDS_BYTES = 163840;
constexpr unsigned OOB = 0x70000000u;
constexpr float CSC = 0.12751743082f;
constexpr float L_LO = 5.9604645e-8f;
constexpr float L_HI = 1.0995116e12f;
constexpr float O_HI = 1.2676506e30f;

constexpr unsigned WS_PART = 32u << 20;
constexpr unsigned WS_MB = 60u << 20;
constexpr unsigned WS_BYTES = 64u << 20;
constexpr int O_SLOT = 17408;
constexpr int O_SCR = 3 * O_SLOT;
constexpr int WO_SLOT = 36864;
typedef unsigned long long u64;
#define GAS __attribute__((address_space(1)))
struct Params {
  const void* x[2]; const void* k[2]; const void* v[2]; const void* wq[2]; const void* wo[2];
  void* o_ws; void* out;
};

DI unsigned cvtpk(float lo, float hi) { f32x2 v = {lo, hi}; bf16x2 b = __builtin_convertvector(v, bf16x2); return __builtin_bit_cast(unsigned, b); }
DI bf16x8 pack8(const f32x16& a, int base) {
  u32x4 w = {cvtpk(a[base + 0], a[base + 1]), cvtpk(a[base + 2], a[base + 3]), cvtpk(a[base + 4], a[base + 5]), cvtpk(a[base + 6], a[base + 7])};
  return __builtin_bit_cast(bf16x8, w);
}
DI bf16x8 pack8s(const f32x16& a, int base, float s) {
  u32x4 w = {cvtpk(a[base + 0] * s, a[base + 1] * s), cvtpk(a[base + 2] * s, a[base + 3] * s), cvtpk(a[base + 4] * s, a[base + 5] * s), cvtpk(a[base + 6] * s, a[base + 7] * s)};
  return __builtin_bit_cast(bf16x8, w);
}
DI float fadd_s(float a, float b) { float r; asm("v_add_f32_e32 %0, %1, %2" : "=v"(r) : "v"(a), "v"(b)); return r; }
DI float max2_s(float a, float b) { return __builtin_elementwise_maximum(a, b); }
DI float swap_max(float v) { auto r = __builtin_amdgcn_permlane32_swap(__float_as_uint(v), __float_as_uint(v), false, false); return max2_s(__uint_as_float(r[0]), __uint_as_float(r[1])); }
DI float swap_sum(float v) { auto r = __builtin_amdgcn_permlane32_swap(__float_as_uint(v), __float_as_uint(v), false, false); return __uint_as_float(r[0]) + __uint_as_float(r[1]); }
DI int opaque_lane() { int l = threadIdx.x; asm volatile("" : "+v"(l)); return l & 63; }
DI u32x4 make_srd(const void* p, unsigned bytes) { const uint64_t a = (uint64_t)p; u32x4 s = {(unsigned)a, (unsigned)(a >> 32) & 0xffffu, bytes, 0x00020000u}; return s; }

template <bool NT> DI void dma1(unsigned m0v, unsigned v, u32x4 srd, unsigned s) {
  if constexpr (NT) asm volatile("s_mov_b32 m0, %0\n\ts_nop 3\n\tbuffer_load_dwordx4 %1, %2, %3 offen nt lds" :: "s"(m0v), "v"(v), "s"(srd), "s"(s) : "memory");
  else asm volatile("s_mov_b32 m0, %0\n\ts_nop 3\n\tbuffer_load_dwordx4 %1, %2, %3 offen lds" :: "s"(m0v), "v"(v), "s"(srd), "s"(s) : "memory");
}
DI void touch1(unsigned m0v, unsigned v, u32x4 srd) {
  asm volatile("s_mov_b32 m0, %0\n\ts_nop 3\n\tbuffer_load_dword %1, %2, 0 offen lds" :: "s"(m0v), "v"(v), "s"(srd) : "memory");
}
DI void dma1_sc1(unsigned m0v, unsigned v, u32x4 srd, unsigned s) {
  asm volatile("s_mov_b32 m0, %0\n\ts_nop 3\n\tbuffer_load_dwordx4 %1, %2, %3 offen sc1 lds" :: "s"(m0v), "v"(v), "s"(srd), "s"(s) : "memory");
}
template <bool SC1> DI void dma_dw(unsigned m0v, unsigned v, u32x4 srd, unsigned s) {
  if constexpr (SC1) asm volatile("s_mov_b32 m0, %0\n\ts_nop 3\n\tbuffer_load_dword %1, %2, %3 offen sc1 lds" :: "s"(m0v), "v"(v), "s"(srd), "s"(s) : "memory");
  else asm volatile("s_mov_b32 m0, %0\n\ts_nop 3\n\tbuffer_load_dword %1, %2, %3 offen lds" :: "s"(m0v), "v"(v), "s"(srd), "s"(s) : "memory");
}
template <int N> DI void wait_vm() { asm volatile("s_waitcnt vmcnt(%0)" :: "i"(N) : "memory"); }
DI void bar() { __builtin_amdgcn_s_waitcnt(0xC07F); asm volatile("s_waitcnt lgkmcnt(0)\n\ts_barrier" ::: "memory"); }
DI bf16x8 lds_b128(unsigned addr) { return *(const LAS bf16x8*)(uintptr_t)addr; }
DI bf16x4 lds_tr(unsigned addr) { return __builtin_amdgcn_ds_read_tr16_b64_v4bf16((LAS bf16x4*)(uintptr_t)addr); }
DI bf16x8 cat(bf16x4 a, bf16x4 b) { return __builtin_shufflevector(a, b, 0, 1, 2, 3, 4, 5, 6, 7); }
#define MFMA32(a, b, c) __builtin_amdgcn_mfma_f32_32x32x16_bf16((a), (b), (c), 0, 0, 0)
#define MFMA16(a, b, c) __builtin_amdgcn_mfma_f32_16x16x32_bf16((a), (b), (c), 0, 0, 0)
DI unsigned ld_mb(const char* p) { return __hip_atomic_load((const GAS unsigned*)p, __ATOMIC_RELAXED, __HIP_MEMORY_SCOPE_AGENT); }
DI void st_mb(char* p, unsigned v) { __hip_atomic_store((GAS unsigned*)p, v, __ATOMIC_RELAXED, __HIP_MEMORY_SCOPE_AGENT); }

#define STAMP(i)

struct Ctx {
  unsigned dva, dvb;
  unsigned dga, dgb;
  unsigned kro[2], vtr[2];
  u32x4 srdKV, srdG;
  unsigned kv_s, g_s;
  int w;
};
DI void issue_kv(const Ctx& c, int i, unsigned lds_tile, unsigned so) {
  dma1<true>(lds_tile + (c.w & 3) * 4096 + i * 1024, (i & 2) ? c.dvb : c.dva, c.srdKV, so + (i & 1) * 128u);
}
template <int TEAM> DI void issue_g(const Ctx& c, unsigned so, int i, unsigned gb) {
  if constexpr (TEAM == 0) dma1<G_NT>(gb + (c.w & 3) * 4096 + i * 1024, (i & 1) ? c.dgb : c.dga, c.srdG, so + (unsigned)i * (8 * 1152));
  else dma1<G_NT>(gb + 16384 + (c.w & 3) * 4096 + i * 1024, (i & 2) ? c.dgb : c.dga, c.srdG, so + (i & 1) * 128u);
}
template <int TEAM> DI unsigned g_off(const Ctx& c, int r, int st) {
  if constexpr (TEAM == 0) return c.g_s + (unsigned)st * 128u; else return c.g_s + (unsigned)st * (64 * 8192) + (unsigned)r * (16 * 256);
}
struct QAddr { unsigned x0, x1, w0, w1; };
DI QAddr q_addr(const Ctx& c, unsigned gb) {
  const int lane = opaque_lane();
  const int r32 = lane & 31, hi = lane >> 5, q4 = (lane & 15) >> 2, pp = lane & 3, blk = (lane >> 4) & 1;
  const int psw = ((pp & 1) << 1) | (pp >> 1);
  const unsigned xrow = 32 * (c.w & 3) + r32;
  const unsigned xb = gb + 1024 * (xrow >> 3) + 64 * (xrow & 7);
  QAddr a;
  a.x0 = xb + 16 * (hi ^ ((xrow >> 2) & 3)); a.x1 = xb + 16 * ((2 + hi) ^ ((xrow >> 2) & 3));
  const unsigned wb = gb + 16384 + 2048 * hi + 64 * q4 + 8 * (psw & 1) + 1024 * (c.w >> 2);
  a.w0 = wb + 16 * ((2 * blk + (psw >> 1)) ^ ((2 * hi) & 3)); a.w1 = wb + 256 + 16 * ((2 * blk + (psw >> 1)) ^ ((2 * hi + 1) & 3));
  asm volatile("" : "+v"(a.x0), "+v"(a.x1), "+v"(a.w0), "+v"(a.w1));
  return a;
}
DI bf16x8 q_rdx(const QAddr& a, int ks) { return lds_b128(((ks & 1) ? a.x1 : a.x0) + 512 * (ks >> 1)); }
DI bf16x8 q_rdw(const QAddr& a, int ks, int cc) { return cat(lds_tr(a.w0 + 4096 * ks + 512 * cc), lds_tr(a.w1 + 4096 * ks + 512 * cc)); }
struct QFrag { bf16x8 xf[4]; bf16x8 wf[4][2]; };
template <int KS> DI void q_read_ks(const QAddr& a, QFrag& f) { f.xf[KS] = q_rdx(a, KS); f.wf[KS][0] = q_rdw(a, KS, 0); f.wf[KS][1] = q_rdw(a, KS, 1); }
template <bool RD, class H> DI void q_pipe(const QAddr& an, QFrag& cur, QFrag& nxt, f32x16 (&qa)[2], H&& hook) {
#define QP(KS) \
  qa[0] = MFMA32(cur.wf[KS][0], cur.xf[KS], qa[0]); \
  if constexpr (RD) q_read_ks<KS>(an, nxt); \
  hook(2 * KS); \
  SB(); \
  qa[1] = MFMA32(cur.wf[KS][1], cur.xf[KS], qa[1]); \
  hook(2 * KS + 1); \
  SB();
  QP(0) QP(1) QP(2) QP(3)
#undef QP
}

template <int V> struct IC { static constexpr int value = V; };
struct AState {
  f32x16 o[4];
  f32x16 sa, sb;
  bf16x8 kf[8];
  bf16x4 vl[8], vh[8];
  u32x4 p0, p1;
  float l0, l1;
};
template <int E> DI void sm_add(AState& a, f32x16& s) { if (E & 1) { a.l1 = fadd_s(a.l1, s[E]); PIN(a.l1); } else { a.l0 = fadd_s(a.l0, s[E]); PIN(a.l0); } }
template <int U> DI void sm_slot(AState& a, f32x16& s) {
  if constexpr (U < 16) { s[U] = __builtin_amdgcn_exp2f(s[U]); PIN(s[U]); }
  if constexpr (U >= 4 && U <= 6) { sm_add<2 * (U - 4)>(a, s); sm_add<2 * (U - 4) + 1>(a, s); }
  if constexpr (U >= 7 && U <= 16) sm_add<U - 1>(a, s);
  if constexpr (U >= 2 && U <= 16 && (U & 1) == 0) {
    constexpr int E = U - 1;
    if constexpr (E < 8) { a.p0[E >> 1] = cvtpk(s[E - 1], s[E]); PIN(a.p0[E >> 1]); }
    else { a.p1[(E - 8) >> 1] = cvtpk(s[E - 1], s[E]); PIN(a.p1[(E - 8) >> 1]); }
  }
}
template <bool LAST = false, class H> DI void phaseA(const Ctx& c, AState& a, f32x16& sn, f32x16& sp, const bf16x8 (&qf)[8], H&& hook) {
  const unsigned v0 = c.vtr[0], v1 = c.vtr[1];
#define VRD(f) do { a.vl[f] = lds_tr(v0 + 4096 * ((f) & 1) + 512 * ((f) >> 1)); a.vh[f] = lds_tr(v1 + 4096 * ((f) & 1) + 512 * ((f) >> 1)); } while (0)
#define GA(i) do { \
    if constexpr (LAST) { } else if constexpr (i == 0) sn = MFMA32(a.kf[0], qf[0], f32x16{}); else sn = MFMA32(a.kf[i], qf[i], sn); \
    if constexpr (EARLY) { if constexpr (i == 0) { VRD(0); } if constexpr (i == 1) { VRD(2); VRD(4); } if constexpr (i == 2) { VRD(6); } if constexpr (i == 3) { VRD(1); VRD(3); } if constexpr (i == 4) { VRD(5); } if constexpr (i == 5) { VRD(7); } } \
    else VRD((2 * (i & 3) + (i >> 2))); \
    sm_slot<4 + i>(a, sp); \
    hook(IC<i>{}); SB(); } while (0)
  GA(0); GA(1); GA(2); GA(3); GA(4); GA(5); GA(6); GA(7);
#undef GA
#undef VRD
}
template <bool ZERO = false, bool LAST = false, class H> DI void phaseB(const Ctx& c, AState& a, f32x16& sn, f32x16& sp, H&& hook) {
  const unsigned k0 = c.kro[0], k1 = c.kro[1];
#define KRD(f) a.kf[f] = lds_b128((((f) & 1) ? k1 : k0) + 512 * ((f) >> 1))
#define GB(i) do { \
    if constexpr (i == 4) sm_slot<16>(a, sp); \
    { constexpr int f = 2 * (i & 3) + (i >> 2); a.o[i & 3] = MFMA32(cat(a.vl[f], a.vh[f]), __builtin_bit_cast(bf16x8, (i < 4) ? a.p0 : a.p1), (ZERO && i < 4) ? f32x16{} : a.o[i & 3]); } \
    if constexpr (LAST) { } else if constexpr (EARLY) { if constexpr (i == 0) { KRD(0); } if constexpr (i == 1) { KRD(1); } if constexpr (i == 2) { KRD(2); KRD(3); } if constexpr (i == 3) { KRD(4); } if constexpr (i == 4) { KRD(5); KRD(6); } if constexpr (i == 5) { KRD(7); } } \
    else KRD(i); \
    if constexpr (i < 4) sm_slot<12 + (i & 3)>(a, sp); else if constexpr (!LAST) sm_slot<(i & 3)>(a, sn); \
    hook(IC<8 + i>{}); SB(); } while (0)
  GB(0); GB(1); GB(2); GB(3); GB(4); GB(5); GB(6); GB(7);
#undef GB
#undef KRD
  PIN(a.l0); PIN(a.l1);
}

template <int TEAM> DI void body(const Params& p, char* lds) {
  const int tid = threadIdx.x, lane = tid & 63;
  const int w = __builtin_amdgcn_readfirstlane(tid >> 6);
  const int qb = w & 3;
  constexpr int team = TEAM;
  const int wg = blockIdx.x, xcd = wg & 7, idx = wg >> 3;
  const int br = xcd >> 2, hq = xcd & 3, b = idx >> 2, hl = idx & 3;
  const unsigned head0 = 4 * hq + hl;
  const unsigned ldsb = (unsigned)(uintptr_t)(LAS char*)lds;
  const unsigned G0 = ldsb + G_OFF, G1 = G0 + G_SLOT;
  const unsigned R0 = ldsb, R1 = R0 + 32768, R2 = R1 + 32768, GX = R2;

  Ctx c;
  c.w = w;
  STAMP(0);
  {
    const unsigned r7 = (lane >> 2) & 7, bit = (lane >> 4) & 1, h4 = 4 * (lane >> 5), l3 = lane & 3;
    if constexpr (team == 0) {
      c.srdG = make_srd(p.x[br], NB * SQ * DM * 2);
      c.dga = r7 * 1152u + (h4 + (l3 ^ bit)) * 16;
      c.dgb = r7 * 1152u + (h4 + (l3 ^ (2 + bit))) * 16;
      c.g_s = (unsigned)b * (SQ * DM * 2) + (unsigned)qb * (32 * 1152);
      c.srdKV = make_srd(p.v[br], NB * NH * SK * DH * 2);
    } else {
      c.srdG = make_srd(p.wq[br], DM * NH * DH * 2);
      c.dga = r7 * 8192u + (h4 + (l3 ^ bit)) * 16;
      c.dgb = (r7 + 8) * 8192u + (h4 + (l3 ^ (2 + bit))) * 16;
      c.g_s = (unsigned)qb * (16 * 8192) + head0 * 256u;
      c.srdKV = make_srd(p.k[br], NB * NH * SK * DH * 2);
    }
    c.dva = r7 * 256u + (h4 + (l3 ^ bit)) * 16;
    c.dvb = (r7 + 8) * 256u + (h4 + (l3 ^ (2 + bit))) * 16;
    c.kv_s = ((unsigned)(b * NH) + head0) * (SK * DH * 2) + (unsigned)qb * (16 * 256);
  }
  const int rot9 = (b + 2 * hl) % 9;
  auto kst = [&](int u) { const int v = u + rot9; return v >= 9 ? v - 9 : v; };
  auto seq_off = [&](int g) -> unsigned { return g < 9 ? g_off<team>(c, 0, kst(g)) : g_off<team>(c, 1, kst(g - 9)); };
#pragma unroll
  for (int i = 0; i < 4; ++i) issue_g<team>(c, seq_off(0), i, GX);
#pragma unroll
  for (int i = 0; i < 4; ++i) issue_g<team>(c, seq_off(1), i, G0);
#pragma unroll
  for (int i = 0; i < 4; ++i) issue_g<team>(c, seq_off(2), i, G1);
  char* const ws = (char*)p.o_ws;
  unsigned nonce;
  { const u64 t = __builtin_amdgcn_s_memrealtime(); nonce = (unsigned)t ^ ((unsigned)(t >> 32) * 0x9E3779B1u); }
  if (tid == 0) { st_mb(ws + WS_MB + wg * 128, nonce); *(LAS unsigned*)(uintptr_t)(R1 + 16384) = nonce; }
  const __amdgpu_buffer_rsrc_t rsW = __builtin_amdgcn_make_buffer_rsrc(ws, 0, WS_BYTES, 0x00020000);
  {
    const int r32 = lane & 31, hi = lane >> 5;
    const int q4 = (lane & 15) >> 2, pp = lane & 3, blk = (lane >> 4) & 1;
    const int psw = ((pp & 1) << 1) | (pp >> 1);
#pragma unroll
    for (int j = 0; j < 2; ++j) {
      const unsigned krow = 32 * team + r32;
      c.kro[j] = 16384 + 2048 * (krow >> 3) + 64 * (krow & 7) + 16 * ((2 * j + hi) ^ ((krow >> 2) & 3));
      c.vtr[j] = 2048 * (4 * team + j) + 64 * (4 * hi + q4) + 16 * ((2 * blk + (psw >> 1)) ^ ((2 * j + hi) & 3)) + 8 * (psw & 1);
    }
  }

  auto q_write = [&](f32x16 (&qa)[2], unsigned gb) {
    const int lane = opaque_lane(); asm volatile("" : "+s"(gb));
#pragma unroll
    for (int i = 0; i < 4; ++i) {
      f32x16 t;
#pragma unroll
      for (int e = 0; e < 8; ++e) t[e] = (float)(__bf16)qa[i >> 1][8 * (i & 1) + e];
      *(LAS bf16x8*)(uintptr_t)(gb + (w * 4 + i) * 1024 + lane * 16) = pack8s(t, 0, CSC);
    }
  };
  auto q_load = [&](bf16x8 (&qf)[8], unsigned gb) {
    const int lane = opaque_lane(); asm volatile("" : "+s"(gb));
#pragma unroll
    for (int s = 0; s < 8; ++s) qf[s] = lds_b128(gb + ((qb + 4 * (s >> 2)) * 4 + (s & 3)) * 1024 + lane * 16);
  };
  bf16x8 fo[4];
  auto merge_store = [&](int r, f32x16 (&o)[4], float m_ref, float l_sum, unsigned gb, unsigned mb) {
    const int lane = opaque_lane(); asm volatile("" : "+s"(gb), "+s"(mb));
    const int r32 = lane & 31, hi = lane >> 5;
    l_sum = swap_sum(l_sum);
    if (hi == 0) { *(LAS f32x2*)(uintptr_t)(mb + (w * 32 + r32) * 8) = f32x2{m_ref, l_sum}; }
#pragma unroll
    for (int i = 0; i < 2; ++i)
#pragma unroll
      for (int g = 0; g < 4; ++g) { const f32x16& s = o[(team ? 0 : 2) + i]; *(LAS f32x4*)(uintptr_t)(gb + w * 8192 + (i * 4 + g) * 1024 + lane * 16) = f32x4{s[4 * g], s[4 * g + 1], s[4 * g + 2], s[4 * g + 3]}; }
    bar();
    const f32x2 ml = *(const LAS f32x2*)(uintptr_t)(mb + ((w ^ 4) * 32 + r32) * 8);
    const float M = fmaxf(m_ref, ml[0]);
    const float a0 = __builtin_amdgcn_exp2f(m_ref - M), a1 = __builtin_amdgcn_exp2f(ml[0] - M);
    const float lt = l_sum * a0 + ml[1] * a1;
    const float inv = 1.0f / lt, sc0 = a0 * inv, sc1 = a1 * inv;
    const unsigned ob = (unsigned)(((br * NB + b) * NH + 16 * r + head0) * 4 + qb) * 8192u;
#pragma unroll
    for (int i = 0; i < 2; ++i) {
      const int cc = 2 * team + i;
      f32x16 f;
#pragma unroll
      for (int g = 0; g < 4; ++g) {
        const f32x4 v = *(const LAS f32x4*)(uintptr_t)(gb + (w ^ 4) * 8192 + (i * 4 + g) * 1024 + lane * 16);
#pragma unroll
        for (int e = 0; e < 4; ++e) { f[4 * g + e] = o[cc][4 * g + e] * sc0 + v[e] * sc1; }
      }
      const bf16x8 g0 = pack8(f, 0), g1 = pack8(f, 8);
      if (r == 1) { fo[2 * i] = g0; fo[2 * i + 1] = g1; }
      __builtin_amdgcn_raw_buffer_store_b128(__builtin_bit_cast(u32x4, g0), rsW, lane * 16 + (2 * cc) * 1024, ob, 16);
      __builtin_amdgcn_raw_buffer_store_b128(__builtin_bit_cast(u32x4, g1), rsW, lane * 16 + (2 * cc + 1) * 1024, ob, 16);
    }
  };
  auto any_bad = [&](unsigned fl) -> bool {
    const int lane = opaque_lane();
    const unsigned f = *(const LAS unsigned*)(uintptr_t)(fl + (lane & 7) * 4);
    return __any(f != 0u);
  };
#define TROT 0
  const int trot = TROT;
  auto tile_off = [&](int T) -> unsigned {
    const unsigned o = c.kv_s + (unsigned)((T + trot) & 15) * 16384u + (T >= 16 ? 16u * SK * DH * 2 : 0u); return T <= 31 ? o : OOB; };

  auto wg_of = [&](int kg_, int hl_) { return (b * 4 + hl_) * 8 + br * 4 + kg_; };
  auto load_tags = [&]() -> unsigned {
    const int l = opaque_lane();
    return ld_mb(ws + WS_MB + ((l & 4) ? wg_of(l & 3, hl) : wg_of(hq, l & 3)) * 128);
  };
  unsigned ptag = 0;
  bf16x8 qf[8];
  f32x16 qa[2] = {};
  AState a;
  {
    QFrag fa, fb;
    const QAddr a0 = q_addr(c, 0);
    auto qaddr = [&](unsigned gb) { QAddr a; a.x0 = a0.x0 + gb; a.x1 = a0.x1 + gb; a.w0 = a0.w0 + gb; a.w1 = a0.w1 + gb; asm volatile("" : "+v"(a.x0), "+v"(a.x1), "+v"(a.w0), "+v"(a.w1)); return a; };
    unsigned gcur = GX, gnxt = G0, gnn = G1;
    auto grot = [&]() { const unsigned tmp = gcur; gcur = gnxt; gnxt = gnn; gnn = tmp; };
    wait_vm<8>();
    bar();
    nonce = __builtin_amdgcn_readfirstlane(*(const LAS unsigned*)(uintptr_t)(R1 + 16384));
    STAMP(8);
    { const QAddr an = qaddr(gcur); q_read_ks<0>(an, fa); q_read_ks<1>(an, fa); q_read_ks<2>(an, fa); q_read_ks<3>(an, fa); }
#define QIT(g, CUR, NXT, NW, EK) do { \
      wait_vm<NW>(); \
      bar(); \
      const QAddr an = qaddr(gnxt); const unsigned so = seq_off((g) + 3); \
      q_pipe<true>(an, CUR, NXT, qa, [&](int i) { if (i < 4) issue_g<team>(c, so, i, gcur); \
        else if constexpr (EK == 1) issue_kv(c, i - 4, team ? R0 : R1, tile_off(0)); \
        else if constexpr (EK == 2 && team == 1) issue_kv(c, i - 4, R0 + 16384, tile_off(1)); }); \
      grot(); } while (0)
    QIT(0, fa, fb, 4, 1); QIT(1, fb, fa, 8, 2); QIT(2, fa, fb, 8 + 4 * team, 0); QIT(3, fb, fa, 4 + 4 * team, 0);
#pragma unroll 1
    for (int g = 4; g < 8; g += 2) { QIT(g, fa, fb, 4, 0); QIT(g + 1, fb, fa, 4, 0); }
#undef QIT
    {
      wait_vm<4>();
      bar();
      const QAddr an = qaddr(gnxt);
      q_pipe<true>(an, fa, fb, qa, [&](int i) { if constexpr (team == 1) { if (i < 4) issue_kv(c, i, R1 + 16384, tile_off(2)); } });
    }
    STAMP(1);
    q_write(qa, G1);
    qa[0] = f32x16{}; qa[1] = f32x16{};
    { const QAddr an = qaddr(gnxt); q_pipe<false>(an, fb, fa, qa, [&](int i) { }); }
  }
  { const int l = opaque_lane();
    asm volatile("global_load_dword %0, %1, off sc1" : "=v"(ptag) : "v"(ws + WS_MB + ((l & 4) ? wg_of(l & 3, hl) : wg_of(hq, l & 3)) * 128) : "memory"); }
  bar();
  q_load(qf, G1);
  {
    unsigned k0 = R0 - 16384 + c.kro[0], k1 = R0 - 16384 + c.kro[1];
    asm volatile("" : "+v"(k0), "+v"(k1));
#pragma unroll
    for (int i = 0; i < 8; ++i) a.kf[i] = lds_b128(((i & 1) ? k1 : k0) + 512 * (i >> 1));
  }
  STAMP(2);
  unsigned pcur = R0, pnxt = R1, pnn = R2;
  c.kro[0] += R0; c.kro[1] += R0; c.vtr[0] += R0; c.vtr[1] += R0;
  auto rot = [&]() { const unsigned d = pnxt - pcur; c.kro[0] += d; c.kro[1] += d; c.vtr[0] += d; c.vtr[1] += d;
    asm volatile("" : "+v"(c.kro[0]), "+v"(c.kro[1]), "+v"(c.vtr[0]), "+v"(c.vtr[1]));
    const unsigned tmp = pcur; pcur = pnxt; pnxt = pnn; pnn = tmp; };
  a.l0 = 0.f; a.l1 = 0.f;
#pragma unroll
  for (int cc = 0; cc < 4; ++cc) a.o[cc] = f32x16{};
  a.sb = f32x16{};
  a.p0 = u32x4{}; a.p1 = u32x4{};

#define KV0 (team ? 4 : 0)
#define GG0 (team ? 11 : 7)
#define KV_HOOK(n) do { if constexpr (n >= KV0 && n < KV0 + 4) issue_kv(c, n - KV0, pnn + (team ? 16384 : 0), offKV); } while (0)
#define STEP_HEAD(T, NW) \
      const unsigned offKV = tile_off((T) + (team ? 3 : 1)); \
      wait_vm<NW>(); \
      bar();
#define STEP_P(T_, SN, SP) do { const int T = (T_); \
      STEP_HEAD(T, 4) \
      phaseA(c, a, SN, SP, qf, [&](auto n) { constexpr int N = decltype(n)::value; KV_HOOK(N); }); \
      phaseB(c, a, SN, SP, [&](auto n) { }); \
      rot(); \
    } while (0)
  QAddr ga = q_addr(c, G0);
#define GRD(ks, xf, wf0, wf1) do { \
          xf = lds_b128(((ks & 1) ? gx1 : gx0) + 512 * (ks >> 1)); \
          wf0 = cat(lds_tr(gw0 + 4096 * ks), lds_tr(gw1 + 4096 * ks)); \
          wf1 = cat(lds_tr(gw0 + 4096 * ks + 512), lds_tr(gw1 + 4096 * ks + 512)); } while (0)
#define STEP_H(T_, PAR, SN, SP) do { const int T = (T_); \
      STEP_HEAD(T, (PAR ? 8 : 4)) \
      const int u = (T >> 1) + 1; \
      const unsigned gi = (u & 1) ? G1 : G0; \
      const unsigned so = u < 8 ? g_off<team>(c, 1, kst(u + 1)) : OOB; \
      const unsigned gx0 = ga.x0, gx1 = ga.x1, gw0 = ga.w0, gw1 = ga.w1; \
      bf16x8 xa, wa0, wa1, xb, wb0, wb1; \
      auto ghook = [&](auto n) { constexpr int N = decltype(n)::value; \
        KV_HOOK(N); \
        if constexpr (PAR == 0 && N >= GG0 && N < GG0 + 4) issue_g<team>(c, so, N - GG0, gi); \
        if constexpr (N == 14) GRD((2 * PAR), xa, wa0, wa1); \
      }; \
      phaseA(c, a, SN, SP, qf, ghook); \
      phaseB(c, a, SN, SP, ghook); \
      GRD((2 * PAR + 1), xb, wb0, wb1); \
      SB(); \
      qa[0] = MFMA32(wa0, xa, qa[0]); qa[1] = MFMA32(wa1, xa, qa[1]); qa[0] = MFMA32(wb0, xb, qa[0]); qa[1] = MFMA32(wb1, xb, qa[1]); \
      SB(); \
      rot(); \
      if constexpr (PAR == 1) { const unsigned d = (u & 1) ? (unsigned)G_SLOT : 0u - (unsigned)G_SLOT; ga.x0 += d; ga.x1 += d; ga.w0 += d; ga.w1 += d; \
        asm volatile("" : "+v"(ga.x0), "+v"(ga.x1), "+v"(ga.w0), "+v"(ga.w1)); } \
    } while (0)

#pragma unroll 1
  for (int T2 = 0; T2 < 16; T2 += 2) {
    STEP_H(T2, 0, a.sa, a.sb);
    if (T2 == 2) { STAMP(9); }
    if (T2 == 0) {
      a.l0 = 0.f; a.l1 = 0.f;
#pragma unroll
      for (int cc = 0; cc < 4; ++cc) a.o[cc] = f32x16{};
    }
    STEP_H(T2 + 1, 1, a.sb, a.sa);
  }
  wait_vm<4>();
  bar();
  q_write(qa, G0);
  bar();
  q_load(qf, G0);
  STAMP(5);
  auto echo = [&](int ph, unsigned tags) {
    const int l = opaque_lane();
    if (l < 4) st_mb(ws + WS_MB + 32768 + wg_of(hq, l) * 128 + ph * 16 + hl * 4, tags);
  };
  auto publish = [&](int ph) {
    if (w == 0) {
      const int l = opaque_lane();
      asm volatile("global_load_dword %0, %1, off sc1\n\ts_waitcnt vmcnt(0)" : "=v"(ptag) : "v"(ws + WS_MB + ((l & 4) ? wg_of(l & 3, hl) : wg_of(hq, l & 3)) * 128) : "memory");
      echo(ph, ptag);
    }
  };
  auto await = [&](int ph) {
    const int l = opaque_lane();
    const char* a = ws + WS_MB + 32768 + wg * 128 + ph * 16 + (l & 3) * 4;
#pragma unroll 1
    for (int spin = 0; spin < 20000; ++spin) {
      const unsigned v = ld_mb(a);
      if (__all(v == nonce)) break;
      __builtin_amdgcn_s_sleep(1);
    }
  };
  auto redo = [&](int r, unsigned fb, unsigned mb) {
    {
      float m = -3.0e38f, l = 0.f;
      bf16x8 qs[8];
      if (r == 0) {
        f32x16 q2[2] = {};
#pragma unroll 1
        for (int st = 0; st < 9; ++st) {
          bar();
#pragma unroll
          for (int i = 0; i < 4; ++i) issue_g<team>(c, g_off<team>(c, 0, st), i, fb);
          wait_vm<0>();
          bar();
          const QAddr ga = q_addr(c, fb);
#pragma unroll
          for (int g = 0; g < 8; ++g) q2[g & 1] = MFMA32(q_rdw(ga, g >> 1, g & 1), q_rdx(ga, g >> 1), q2[g & 1]);
        }
        bar();
        q_write(q2, fb);
        bar();
        q_load(qs, fb);
      } else {
#pragma unroll
        for (int i = 0; i < 8; ++i) qs[i] = qf[i];
      }
#pragma unroll
      for (int cc = 0; cc < 4; ++cc) a.o[cc] = f32x16{};
#pragma unroll 1
      for (int t = 0; t < 16; ++t) {
        bar();
        {
          const unsigned o = c.kv_s + (unsigned)r * (16u * SK * DH * 2) + (unsigned)t * 16384u;
#pragma unroll
          for (int i = 0; i < 4; ++i) issue_kv(c, i, fb + (team ? 16384 : 0), o);
          wait_vm<0>();
        }
        bar();
        const unsigned dd = fb - pcur;
        unsigned k0 = dd + c.kro[0], k1 = dd + c.kro[1], v0 = dd + c.vtr[0], v1 = dd + c.vtr[1];
        asm volatile("" : "+v"(k0), "+v"(k1), "+v"(v0), "+v"(v1));
        f32x16 s = {};
#pragma unroll
        for (int i = 0; i < 8; ++i) s = MFMA32(lds_b128(((i & 1) ? k1 : k0) + 512 * (i >> 1)), qs[i], s);
        float mx = s[0];
#pragma unroll
        for (int e = 1; e < 16; ++e) mx = max2_s(mx, s[e]);
        mx = swap_max(mx);
        const float mn = max2_s(m, mx);
        const float f = __builtin_amdgcn_exp2f(m - mn);
        m = mn; l *= f;
#pragma unroll
        for (int e = 0; e < 16; ++e) { s[e] = __builtin_amdgcn_exp2f(s[e] - mn); l += s[e]; }
        const bf16x8 q0 = pack8(s, 0), q1 = pack8(s, 8);
#pragma unroll
        for (int cc = 0; cc < 4; ++cc) {
#pragma unroll
          for (int e = 0; e < 16; ++e) a.o[cc][e] *= f;
#pragma unroll
          for (int h = 0; h < 2; ++h) {
            const int fr = 2 * cc + h;
            a.o[cc] = MFMA32(cat(lds_tr(v0 + 4096 * (fr & 1) + 512 * (fr >> 1)), lds_tr(v1 + 4096 * (fr & 1) + 512 * (fr >> 1))), h ? q1 : q0, a.o[cc]);
          }
        }
      }
      bar();
      merge_store(r, a.o, m, l, fb, mb);
      wait_vm<0>();
      bar();
    }
  };
  auto seq_head = [&](int q) {
    return q < 2 ? 16 * q + 4 * hq + hl : (q <= 4 ? 4 * hq + ((hl + q - 1) & 3) : 16 + 4 * hq + ((hl + q - 4) & 3));
  };
  auto wo_slot = [&](int q) -> unsigned { return (q & 3) == 0 ? 124u * 1024u : (unsigned)((q & 3) - 1) * WO_SLOT; };
  constexpr unsigned T_X = 36 * 1024, T_L = 108 * 1024, T_FL = 109 * 1024, T_MB = 110 * 1024;
  unsigned woa, wob, woe;
  u32x4 srdO;
  auto wo_piece = [&](int q, int i) {
    const unsigned sl = ldsb + wo_slot(q);
    const unsigned so = (unsigned)seq_head(q) * (128u * 1152u);
    if (i < 4) dma1<false>(sl + team * 16384 + qb * 4096 + i * 1024, (i & 2) ? wob : woa, srdO, so + (unsigned)(64 * team + 16 * qb) * 1152u + hl * 256u + (i & 1) * 128u);
    else if constexpr (team == 0) dma1<false>(sl + 32768 + qb * 1024, woe, srdO, so + (unsigned)(32 * qb) * 1152u + 1024u + hl * 32u);
  };
  auto wo_issue = [&](int q) {
#pragma unroll
    for (int i = 0; i < 5; ++i) wo_piece(q, i);
  };
  f32x16 oh[2];
  float lown = 0.f, chk = 0.f, inv = 0.f;
  bool lok = true;
  f32x4 mt[3];
  auto mrg_a = [&](unsigned xa) {
    const int lane = opaque_lane(); asm volatile("" : "+s"(xa));
    lown = swap_sum(a.l0 + a.l1);
#pragma unroll
    for (int i = 0; i < 2; ++i) {
#pragma unroll
      for (int g = 0; g < 4; ++g) { const f32x16& t = a.o[(team ? 0 : 2) + i]; *(LAS f32x4*)(uintptr_t)(xa + w * 8192 + (i * 4 + g) * 1024 + lane * 16) = f32x4{t[4 * g], t[4 * g + 1], t[4 * g + 2], t[4 * g + 3]}; }
      oh[i] = a.o[2 * team + i];
    }
  };
  auto mrg_b = [&](int n, unsigned xr) {
    if (n < 8) mt[n % 3] = *(const LAS f32x4*)(uintptr_t)(xr + n * 1024);
    if (n >= 2) { const int k = n - 2;
#pragma unroll
      for (int e = 0; e < 4; ++e) { float t = fadd_s(oh[k >> 2][4 * (k & 3) + e], mt[k % 3][e]); PIN(t); oh[k >> 2][4 * (k & 3) + e] = t; } }
  };
  auto mrg_inv = [&](float lp) { const float lt = lown + lp; inv = 1.0f / lt; lok = (lt >= L_LO) && (lt <= L_HI); chk = 0.f; };
  auto mrg_c = [&](int r, int k, unsigned vo) {
    f32x16 f;
#pragma unroll
    for (int e = 0; e < 8; ++e) { f[e] = oh[k >> 1][8 * (k & 1) + e] * inv; chk += __builtin_fabsf(f[e]); }
    const bf16x8 g = pack8(f, 0);
    if (r == 1) fo[k] = g;
    __builtin_amdgcn_raw_buffer_store_b128(__builtin_bit_cast(u32x4, g), rsW, vo + (4 * team + k) * 1024, (unsigned)(((br * NB + b) * NH + 16 * r + head0) * 4 + qb) * 8192u, 16);
  };
  auto mrg_flag = [&](unsigned fl) {
    const unsigned bad = __any(!(lok && chk <= O_HI)) ? 1u : 0u;
    if (opaque_lane() == 0) *(LAS unsigned*)(uintptr_t)(fl + w * 4) = bad;
  };
  auto wo_touch = [&]() {
    const int lane = opaque_lane();
    const u32x4 srdT = make_srd(p.wo[br], NH * DH * DM * 2);
#pragma unroll
    for (int h = 0; h < 2; ++h) {
      const unsigned L = (qb * 2 + h) * 48 + min(lane, 47), ri = L / 3, wh = L % 3, rr = ri * 8 + b;
      dma_dw<false>(G0 + 4096 + qb * 256, ((rr >> 9) * 2048u + 512u * hq + (rr & 511)) * 1152u + (wh < 2 ? hl * 256u + wh * 128u : 1024u + hl * 32u), srdT, 0u);
    }
  };
#define STEP_X(T_, NW, SN, SP, ZERO, LAST, PRE, HOOK) do { const int T = (T_); \
      const unsigned offKV = tile_off((T) + (team ? 3 : 1)); \
      wait_vm<NW>(); \
      bar(); \
      PRE; \
      auto xh = [&](auto n) { constexpr int N = decltype(n)::value; HOOK; }; \
      phaseA<LAST>(c, a, SN, SP, qf, xh); \
      phaseB<ZERO, LAST>(c, a, SN, SP, xh); \
      rot(); \
    } while (0)
  STEP_P(16, a.sa, a.sb);
  STAMP(3);
  mrg_a(G0);
  a.l0 = 0.f; a.l1 = 0.f;
  {
    const int lane = opaque_lane();
    unsigned xr = G0 + (w ^ 4) * 8192 + lane * 16;
    unsigned lw = pnn + (team ? 0 : 16384) + qb * 4096 + 3072 + (lane & 31) * 4;
    asm volatile("" : "+v"(xr), "+v"(lw));
    STEP_X(17, 4, a.sb, a.sa, true, false, (*(LAS float*)(uintptr_t)lw = lown),
      { if constexpr (N >= KV0 && N < KV0 + 3) issue_kv(c, N - KV0, pnn + (team ? 16384 : 0), offKV);
        if constexpr (N >= 3 && N < 13) mrg_b(N - 3, xr); });
  }
  {
    const int lane = opaque_lane();
    unsigned lr = pnxt + (team ? 16384 : 0) + qb * 4096 + 3072 + (lane & 31) * 4;
    unsigned vo = lane * 16;
    asm volatile("" : "+v"(lr), "+v"(vo));
    const unsigned offD = tile_off(17 + (team ? 3 : 1));
    float lp;
    STEP_X(18, 3, a.sa, a.sb, false, false, (lp = *(const LAS float*)(uintptr_t)lr),
      { if constexpr (N == 1) { mrg_inv(lp); PIN(lp); issue_kv(c, 3, pnxt + (team ? 16384 : 0), offD); }
        if constexpr (N >= 2 && N < 6) mrg_c(0, N - 2, vo);
        if constexpr (N == 6) mrg_flag(G0);
        if constexpr (N == 7 && team == 1) wo_touch();
        if constexpr (N >= 8 && N < 12) issue_kv(c, N - 8, pnn + (team ? 16384 : 0), offKV); });
  }
  STAMP(4);
  unsigned e0 = 0;
  bool bad0 = false;
#pragma unroll 1
  for (int T2 = 19; T2 < 31; T2 += 2) {
    if (T2 == 21) { bad0 = any_bad(G0); if (w == 0 && !bad0) echo(0, ptag); }
    if (T2 == 27) asm volatile("global_load_dword %0, %1, off sc1" : "+v"(e0) : "v"(ws + WS_MB + 32768 + wg * 128 + (opaque_lane() & 3) * 4) : "memory");
    STEP_P(T2, a.sb, a.sa);
    STEP_P(T2 + 1, a.sa, a.sb);
  }
  constexpr int NX = team ? 4 : 5;
  const u32x4 srdWS = make_srd(ws, WS_BYTES);
  unsigned fvo;
  {
    const int lane = opaque_lane();
    const unsigned r7 = (lane >> 2) & 7, bit = (lane >> 4) & 1, h4 = 4 * (lane >> 5), l3 = lane & 3;
    srdO = make_srd(p.wo[br], NH * DH * DM * 2);
    woa = r7 * 1152u + (h4 + (l3 ^ bit)) * 16;
    wob = (r7 + 8) * 1152u + (h4 + (l3 ^ (2 + bit))) * 16;
    woe = (lane >> 1) * 1152u + (lane & 1) * 16;
    fvo = lane * 16 + qb * 8192 + team * 4096;
    asm volatile("" : "+v"(woa), "+v"(wob), "+v"(woe), "+v"(fvo));
  }
  auto f_one = [&](int q, int i, bf16x8& dst) {
    const unsigned so = (unsigned)((br * NB + b) * NH + seq_head(q)) * 32768u;
    if (i == 0) asm volatile("s_nop 4\n\tbuffer_load_dwordx4 %0, %1, %2, %3 offen sc1" : "=v"(dst) : "v"(fvo), "s"(srdWS), "s"(so) : "memory");
    if (i == 1) asm volatile("s_nop 4\n\tbuffer_load_dwordx4 %0, %1, %2, %3 offen offset:1024 sc1" : "=v"(dst) : "v"(fvo), "s"(srdWS), "s"(so) : "memory");
    if (i == 2) asm volatile("s_nop 4\n\tbuffer_load_dwordx4 %0, %1, %2, %3 offen offset:2048 sc1" : "=v"(dst) : "v"(fvo), "s"(srdWS), "s"(so) : "memory");
    if (i == 3) asm volatile("s_nop 4\n\tbuffer_load_dwordx4 %0, %1, %2, %3 offen offset:3072 sc1" : "=v"(dst) : "v"(fvo), "s"(srdWS), "s"(so) : "memory");
  };
  bf16x8 FA[4], FB[4], FC[4], FD[4];
  STEP_X(31, 4, a.sb, a.sa, false, false, (void)0,
    { (void)offKV; if constexpr (N >= 1 && N < 6) wo_piece(0, N - 1); if constexpr (N >= 8 && N < 12) f_one(0, N - 8, FA[N - 8]); });
  STEP_X(32, NX + 4, a.sa, a.sb, false, true, (void)0,
    { (void)offKV; if constexpr (N >= 1 && N < 6) wo_piece(1, N - 1); });
  STAMP(6);
  {
    f32x16 om[4] = {};
    f32x4 oe[2] = {};
    const int lane = opaque_lane();
    const QAddr g0 = q_addr(c, 0);
    const unsigned wl0 = ldsb + g0.w0 - 16384 - 1024 * team + 16384 * team, wl1 = ldsb + g0.w1 - 16384 - 1024 * team + 16384 * team;
    const unsigned c4 = lane >> 4, i16 = lane & 15;
    const unsigned we = ldsb + 32768 + (64 * team + 16 * (c4 & 1) + 8 * (c4 >> 1) + (i16 >> 2)) * 32 + (i16 & 3) * 8;
#define WAIT_F(N, F) asm volatile("s_waitcnt vmcnt(%4)" : "+v"(F[0]), "+v"(F[1]), "+v"(F[2]), "+v"(F[3]) : "i"(N) : "memory")
    auto group = [&](int q, const bf16x8 (&F)[4], auto&& hook) {
      const unsigned sl = wo_slot(q);
      unsigned a0 = wl0 + sl, a1 = wl1 + sl, ae = we + sl;
      asm volatile("" : "+v"(a0), "+v"(a1), "+v"(ae));
#pragma unroll
      for (int ks = 0; ks < 4; ++ks)
#pragma unroll
        for (int nb = 0; nb < 4; ++nb) {
          om[nb] = MFMA32(cat(lds_tr(a0 + 4096 * ks + 512 * nb), lds_tr(a1 + 4096 * ks + 512 * nb)), F[ks], om[nb]);
          if (nb & 1) { hook(2 * ks + (nb >> 1)); SB(); }
        }
#pragma unroll
      for (int kk = 0; kk < 2; ++kk) {
        u32x4 f0 = __builtin_bit_cast(u32x4, F[2 * kk]), f1 = __builtin_bit_cast(u32x4, F[2 * kk + 1]);
#pragma unroll
        for (int e = 0; e < 4; ++e) { auto r = __builtin_amdgcn_permlane16_swap(f0[e], f1[e], false, false); f0[e] = r[0]; f1[e] = r[1]; }
        const bf16x8 ax = cat(lds_tr(ae + 1024 * kk), lds_tr(ae + 1024 * kk + 128));
        oe[0] = MFMA16(ax, __builtin_bit_cast(bf16x8, f0), oe[0]);
        oe[1] = MFMA16(ax, __builtin_bit_cast(bf16x8, f1), oe[1]);
      }
    };
    bar();
#pragma unroll
    for (int i = 0; i < 4; ++i) f_one(2, i, FB[i]);
#pragma unroll
    for (int i = 0; i < 4; ++i) f_one(3, i, FC[i]);
    mrg_a(ldsb + T_X);
    if (lane < 32) *(LAS float*)(uintptr_t)(ldsb + T_L + w * 128 + lane * 4) = lown;
    WAIT_F(NX + 8, FA);
    bar();
    {
      unsigned xr = ldsb + T_X + (w ^ 4) * 8192 + lane * 16, vo = lane * 16;
      asm volatile("" : "+v"(xr), "+v"(vo));
      mrg_inv(*(const LAS float*)(uintptr_t)(ldsb + T_L + (w ^ 4) * 128 + (lane & 31) * 4));
      mrg_b(0, xr); mrg_b(1, xr);
      group(0, FA, [&](int i) {
        if (i < 4) { mrg_b(2 * i + 2, xr); mrg_b(2 * i + 3, xr); }
        if (i >= 2 && i < 6) mrg_c(1, i - 2, vo);
        if (i == 6) mrg_flag(ldsb + T_FL); });
    }
    STAMP(7);
    wait_vm<12>();
    bar();
    if (bad0 | any_bad(ldsb + T_FL)) {
      const bool bad1 = any_bad(ldsb + T_FL);
      if (bad1) redo(1, ldsb + T_X, ldsb + T_MB);
      if (bad0) { redo(0, ldsb + T_X, ldsb + T_MB); publish(0); }
#pragma unroll
      for (int nb = 0; nb < 4; ++nb) om[nb] = f32x16{};
      oe[0] = f32x4{}; oe[1] = f32x4{};
      wo_issue(0);
#pragma unroll
      for (int i = 0; i < 4; ++i) f_one(0, i, FA[i]);
      WAIT_F(0, FA);
      bar();
      group(0, FA, [&](int) {});
      bar();
    }
    if (!__all(e0 == nonce)) {
      await(0); publish(0);
      WAIT_F(0, FB); WAIT_F(0, FC);
#pragma unroll
      for (int i = 0; i < 4; ++i) f_one(2, i, FB[i]);
#pragma unroll
      for (int i = 0; i < 4; ++i) f_one(3, i, FC[i]);
      WAIT_F(0, FB); WAIT_F(0, FC);
    }
    group(1, fo, [&](int i) { if (i < 5) wo_piece(2, i); if (i >= 2 && i < 6) f_one(4, i - 2, FD[i - 2]); if (i >= 3) wo_piece(3, i - 3); });
    WAIT_F(team ? 7 : 6, FB);
    bar();
    if (w == 0) echo(1, ptag);
    group(2, FB, [&](int i) { if (i < 5) wo_piece(4, i); });
    STAMP(13);
    WAIT_F(NX, FC);
    bar();
    unsigned e1;
    asm volatile("global_load_dword %0, %1, off sc1" : "=v"(e1) : "v"(ws + WS_MB + 32768 + wg * 128 + 16 + (lane & 3) * 4) : "memory");
    unsigned ptag2;
    asm volatile("global_load_dword %0, %1, off sc1" : "=v"(ptag2) : "v"(ws + WS_MB + ((lane & 4) ? wg_of(lane & 3, hl) : wg_of(hq, lane & 3)) * 128) : "memory");
    group(3, FC, [&](int i) { if (i < 5) wo_piece(5, i); });
    asm volatile("s_waitcnt vmcnt(%6)" : "+v"(FD[0]), "+v"(FD[1]), "+v"(FD[2]), "+v"(FD[3]), "+v"(e1), "+v"(ptag2) : "i"(NX) : "memory");
    bar();
    if (w == 0 && __any((lane & 60) == 0 && ptag2 != ptag)) { ptag = ptag2; echo(0, ptag); echo(1, ptag); }
    if (!__all(e1 == nonce)) await(1);
    group(4, FD, [&](int i) { if (i < 4) f_one(5, i, FA[i]); else f_one(6, i - 4, FB[i - 4]); if (i >= 3) wo_piece(6, i - 3); });
    WAIT_F(4 + NX, FA);
    bar();
    group(5, FA, [&](int i) { if (i < 4) f_one(7, i, FC[i]); if (i >= 3) wo_piece(7, i - 3); });
    WAIT_F(4 + NX, FB);
    bar();
    group(6, FB, [&](int) {});
    WAIT_F(0, FC);
    bar();
    group(7, FC, [&](int) {});
    STAMP(10);
    {
      const unsigned xb = ldsb + w * 9216 + lane * 16;
#pragma unroll
      for (int i = 0; i < 2; ++i)
#pragma unroll
        for (int g = 0; g < 4; ++g) { const f32x16& t = om[(team ? 0 : 2) + i]; *(LAS f32x4*)(uintptr_t)(xb + (i * 4 + g) * 1024) = f32x4{t[4 * g], t[4 * g + 1], t[4 * g + 2], t[4 * g + 3]}; }
      *(LAS f32x4*)(uintptr_t)(xb + 8192) = oe[team ? 0 : 1];
    }
    bar();
    f32x16 r2[2]; f32x4 re;
    {
      const unsigned xb = ldsb + (w ^ 4) * 9216 + lane * 16;
#pragma unroll
      for (int i = 0; i < 2; ++i)
#pragma unroll
        for (int g = 0; g < 4; ++g) { const f32x4 v = *(const LAS f32x4*)(uintptr_t)(xb + (i * 4 + g) * 1024);
#pragma unroll
          for (int e = 0; e < 4; ++e) r2[i][4 * g + e] = om[2 * team + i][4 * g + e] + v[e]; }
      re = oe[team] + *(const LAS f32x4*)(uintptr_t)(xb + 8192);
    }
    const unsigned pbase = WS_PART + (unsigned)(((br * NB + b) * 4 + hl) * 16) * (8u * 3072u) + (unsigned)w * 3072u;
    auto send = [&]() {
#pragma unroll
      for (int k = 0; k < 4; ++k) {
        if (k != hq) {
          const unsigned so = pbase + (unsigned)(k * 4 + hq) * (8u * 3072u);
          const unsigned tg = __builtin_amdgcn_readlane(ptag, 4 + k);
          const f32x16& t = r2[k >> 1];
          const int o8 = 8 * (k & 1);
          __builtin_amdgcn_raw_buffer_store_b128(u32x4{__float_as_uint(t[o8]), __float_as_uint(t[o8 + 1]), __float_as_uint(t[o8 + 2]), tg}, rsW, lane * 16, so, 16);
          __builtin_amdgcn_raw_buffer_store_b128(u32x4{__float_as_uint(t[o8 + 3]), __float_as_uint(t[o8 + 4]), __float_as_uint(t[o8 + 5]), tg}, rsW, lane * 16 + 1024, so, 16);
          __builtin_amdgcn_raw_buffer_store_b128(u32x4{__float_as_uint(t[o8 + 6]), __float_as_uint(t[o8 + 7]), __float_as_uint(re[k]), tg}, rsW, lane * 16 + 2048, so, 16);
        }
      }
    };
    send();
    STAMP(11);
    float u[9];
    {
      f32x16 t = hq & 2 ? r2[1] : r2[0];
#pragma unroll
      for (int e = 0; e < 8; ++e) u[e] = (hq & 1) ? t[8 + e] : t[e];
      u[8] = hq == 0 ? re[0] : (hq == 1 ? re[1] : (hq == 2 ? re[2] : re[3]));
    }
    float s9[9];
    unsigned t2 = ptag;
#pragma unroll 1
    for (int spin = 0; spin < 4000; ++spin) {
#pragma unroll 1
      for (int sp2 = 0; sp2 < 4000; ++sp2) {
        const int k = lane & 3;
        unsigned tg = nonce;
        if (lane < 4 && k != hq) tg = __builtin_amdgcn_raw_buffer_load_b32(rsW, pbase + (unsigned)(hq * 4 + k) * (8u * 3072u) + 2048 + 63 * 16 + 12, 0, 16);
        if (__all(tg == nonce)) break;
        __builtin_amdgcn_s_sleep(1);
      }
      bool ok = true;
      t2 = load_tags();
#pragma unroll
      for (int e = 0; e < 9; ++e) s9[e] = 0.f;
#pragma unroll
      for (int k = 0; k < 4; ++k) {
        if (k == hq) {
#pragma unroll
          for (int e = 0; e < 9; ++e) s9[e] += u[e];
        } else {
          const unsigned so = pbase + (unsigned)(hq * 4 + k) * (8u * 3072u);
#pragma unroll
          for (int g = 0; g < 3; ++g) {
            const u32x4 v = __builtin_amdgcn_raw_buffer_load_b128(rsW, lane * 16 + g * 1024, so, 16);
            ok &= v[3] == nonce;
            s9[3 * g] += __uint_as_float(v[0]); s9[3 * g + 1] += __uint_as_float(v[1]); s9[3 * g + 2] += __uint_as_float(v[2]);
          }
        }
      }
      if (__all(ok)) break;
    }
    if (__any((lane & 60) == 4 && t2 != ptag)) { ptag = t2; send(); }
    const __amdgpu_buffer_rsrc_t rsO = __builtin_amdgcn_make_buffer_rsrc(p.out, 0, NB * SQ * 2 * DM * 2, 0x00020000);
    {
      const int row = b * SQ + 32 * qb + (lane & 31);
      const int n0 = br * DM + 128 * hl + 32 * (2 * team + (hq >> 1)) + 16 * (hq & 1) + 8 * (lane >> 5);
      u32x4 pk = {cvtpk(s9[0], s9[1]), cvtpk(s9[2], s9[3]), cvtpk(s9[4], s9[5]), cvtpk(s9[6], s9[7])};
      __builtin_amdgcn_raw_buffer_store_b128(pk, rsO, (unsigned)(row * (2 * DM) + n0) * 2u, 0, OAUX);
      const int rowe = b * SQ + 32 * qb + 16 * team + (lane & 15);
      __builtin_amdgcn_raw_buffer_store_b16((unsigned short)(cvtpk(s9[8], s9[8]) & 0xffffu), rsO, (unsigned)(rowe * (2 * DM) + br * DM + 512 + 16 * hl + 4 * (lane >> 4) + hq) * 2u, 0, OAUX);
    }
    STAMP(12);
  }
}

__global__ __launch_bounds__(512, 2) void attn_kernel(Params p) {
  extern __shared__ __attribute__((aligned(16))) char lds[];
  if (__builtin_amdgcn_readfirstlane(threadIdx.x >> 8) == 0) body<0>(p, lds); else body<1>(p, lds);
}

extern "C" void kernel_launch(void* const* d_in, const int* in_sizes, int n_in, void* d_out, int out_size, void* d_ws, size_t ws_size, hipStream_t stream) {
  Params p;
  for (int i = 0; i < 2; ++i) { p.x[i] = d_in[5 * i + 0]; p.k[i] = d_in[5 * i + 1]; p.v[i] = d_in[5 * i + 2]; p.wq[i] = d_in[5 * i + 3]; p.wo[i] = d_in[5 * i + 4]; }
  p.o_ws = d_ws; p.out = d_out;
  (void)hipFuncSetAttribute((const void*)attn_kernel, hipFuncAttributeMaxDynamicSharedMemorySize, LDS_BYTES);
  hipLaunchKernelGGL(attn_kernel, dim3(256), dim3(512), LDS_BYTES, stream, p);
}
```
